```python
import torch

def _softmax(x, axis=-1):
    return torch.softmax(x, dim=axis)
def _gelu(x):
    return torch.nn.functional.gelu(x, approximate="tanh")
def _silu(x):
    return torch.nn.functional.silu(x)
def _sigmoid(x):
    return torch.sigmoid(x)
def _layernorm(x, axis=-1, eps=1e-5):
    m = x.mean(dim=axis, keepdim=True)
    v = x.var(dim=axis, keepdim=True, unbiased=False)
    return (x - m) / torch.sqrt(v + eps)
def _scatter_add(tgt, idx, src, axis=0):
    return tgt.clone().index_add_(axis, idx, src)
def _segment_sum(src, seg, n):
    out = torch.zeros((n,) + src.shape[1:], dtype=src.dtype)
    return out.index_add_(0, seg, src)
def _pad(x, pw):
    flat = tuple(v for pair in reversed(pw) for v in pair)
    return torch.nn.functional.pad(x, flat)

kernel_name = 'parallel_sum__sliding_block__glu_gated_bilinear__prompt'

def setup_inputs():

    _w = (lambda a: (lambda t: t.to(torch.bfloat16) if t.is_floating_point() else t)(torch.as_tensor(a)))
    def _A_setup():
        B, S, D = 1, 2048, 512
        return {
            "x": _w((lambda *s: torch.randn(*s, dtype=torch.bfloat16) * 0.05)(B, S, D)),
            "wqkv": _w((lambda *s: torch.randn(*s, dtype=torch.bfloat16) * 0.05)(D, 3 * D)),
            "wo": _w((lambda *s: torch.randn(*s, dtype=torch.bfloat16) * 0.05)(D, D)),
        }
    _A_in = _A_setup()

    def _B_setup():
        B, S, D, F = 1, 2048, 512, 1792
        return {
            "x": _w((lambda *s: torch.randn(*s, dtype=torch.bfloat16) * 0.05)(B, S, D)),
            "w_gate": _w((lambda *s: torch.randn(*s, dtype=torch.bfloat16) * 0.8)(D, F) * (D ** -0.5)),
            "w_up": _w((lambda *s: torch.randn(*s, dtype=torch.bfloat16) * 0.8)(D, F) * (D ** -0.5)),
            "w_down": _w((lambda *s: torch.randn(*s, dtype=torch.bfloat16) * 0.8)(F, D) * (F ** -0.5)),
        }
    _B_in = _B_setup()

    return {
        "A_x": _A_in["x"],
        "A_wqkv": _A_in["wqkv"],
        "A_wo": _A_in["wo"],
        "B_x": _B_in["x"],
        "B_w_gate": _B_in["w_gate"],
        "B_w_up": _B_in["w_up"],
        "B_w_down": _B_in["w_down"],
    }

def reference(A_x, A_wqkv, A_wo, B_x, B_w_gate, B_w_up, B_w_down):
    def _A_ref(x, wqkv, wo):
        B, S, D = x.shape
        H = 32; Dh = D // H; W = 256
        qkv = (lambda x, *d: x.permute(*d))((x @ wqkv).reshape(B, S, 3, H, Dh), 2, 0, 3, 1, 4)
        q, k, v = qkv[0], qkv[1], qkv[2]
        scores = torch.einsum("bhqd,bhkd->bhqk", q, k) * (Dh ** -0.5)
        idx = torch.arange(S)
        mask = (idx[:, None] - idx[None, :] >= 0) & (idx[:, None] - idx[None, :] < W)
        scores = torch.where(mask[None, None], scores, -1e9)
        p = _softmax(scores, axis=-1)
        o = torch.einsum("bhqk,bhkd->bhqd", p, v)
        return (lambda x, *d: x.permute(*d))(o, 0, 2, 1, 3).reshape(B, S, D) @ wo

    def _B_ref(x, w_gate, w_up, w_down):
        g = torch.einsum("bsd,df->bsf", x, w_gate)
        u = torch.einsum("bsd,df->bsf", x, w_up)
        h = g * u
        return torch.einsum("bsf,fd->bsd", h, w_down)

    x = A_x
    xn = _layernorm(x, axis=-1)
    return x + _A_ref(xn, A_wqkv, A_wo) + _B_ref(xn, B_w_gate, B_w_up, B_w_down)
```

```cpp
#include <hip/hip_runtime.h>
#include <hip/hip_bf16.h>
#include <type_traits>
#include <stdint.h>

#define LAS __attribute__((address_space(3)))
#define GAS __attribute__((address_space(1)))
typedef unsigned short u16;
typedef unsigned long long u64;
typedef short bf16x8 __attribute__((ext_vector_type(8)));
typedef short s16x4 __attribute__((ext_vector_type(4)));
typedef float f32x16 __attribute__((ext_vector_type(16)));
typedef float f32x4 __attribute__((ext_vector_type(4)));
typedef float f32x2 __attribute__((ext_vector_type(2)));
typedef __bf16 bf16x2 __attribute__((ext_vector_type(2)));
typedef unsigned u32x4 __attribute__((ext_vector_type(4)));
typedef unsigned u32x2 __attribute__((ext_vector_type(2)));
typedef __amdgpu_buffer_rsrc_t rsrc_t;

constexpr int S_ = 2048, D_ = 512, F_ = 1792;
constexpr unsigned WS_EPOCH = 0;
constexpr unsigned WS_FLAGA = 4096;
constexpr unsigned WS_TMO = 16384;
constexpr unsigned WS_FLAGB = 32768;
constexpr unsigned WS_FLAGO = 49152;
constexpr unsigned WS_STATS = 65536;
constexpr unsigned WS_CSA = 131072;
constexpr unsigned WS_CSB = 262144;
constexpr unsigned WS_Q = 524288;
constexpr unsigned WS_K = WS_Q + 2097152;
constexpr unsigned WS_V = WS_K + 2097152;
constexpr unsigned WS_ACT = WS_V + 2097152;
constexpr unsigned WS_END = WS_ACT + 64u * 144u * 1024u;
constexpr int SLOT = 28800, UOFF = 14336 + 128, NSLOT = 5;
constexpr int L_G = NSLOT * SLOT;
constexpr int L_ST = L_G + 16384;
constexpr int L_CS = L_ST + 2048;
constexpr int L_END = L_CS + 1280;
constexpr int LK = 0, LV = 16384;
static_assert(L_END <= 163840, "LDS");

#define STAMP(k) do {} while (0)
#define STAMP_DECL do {} while (0)
#define STAMP_FLUSH() do {} while (0)
#define ST_ARG
#define ST_PASS

struct Params {
  const u16* x; const u16* wqkv; const u16* wo; const u16* wg; const u16* wu; const u16* wd;
  u16* out; char* ws;
};

template <int I, int N, class Fn> __device__ __forceinline__ void static_for(Fn&& f) {
  if constexpr (I < N) { f(std::integral_constant<int, I>{}); static_for<I + 1, N>(f); }
}
template <int V> using ic = std::integral_constant<int, V>;
__device__ __forceinline__ unsigned rfl(unsigned v) { return __builtin_amdgcn_readfirstlane(v); }
__device__ __forceinline__ float bf2f(unsigned b16) { return __uint_as_float(b16 << 16); }
__device__ __forceinline__ unsigned pk2(float lo, float hi) { f32x2 v = {lo, hi}; return __builtin_bit_cast(unsigned, __builtin_convertvector(v, bf16x2)); }
__device__ __forceinline__ unsigned f2bf(float f) { return pk2(f, 0.f) & 0xffffu; }
__device__ __forceinline__ float rbf(float f) { return bf2f(f2bf(f)); }
#define MFMA(a, b, c) __builtin_amdgcn_mfma_f32_32x32x16_bf16(a, b, c, 0, 0, 0)
#define SB0() __builtin_amdgcn_sched_barrier(0)
#define SGB(mask, n) __builtin_amdgcn_sched_group_barrier(mask, n, 0)
__device__ __forceinline__ rsrc_t mkrs(const void* p, unsigned bytes) {
  const u64 a = (u64)p;
  const u64 u = ((u64)rfl((unsigned)(a >> 32)) << 32) | rfl((unsigned)a);
  return __builtin_amdgcn_make_buffer_rsrc((void*)u, (short)0, (int)rfl(bytes), 0x00020000);
}
__device__ __forceinline__ void dma16(rsrc_t rs, unsigned ldsaddr, unsigned voff, unsigned soff) {
  asm volatile("s_mov_b32 m0, %0\n\ts_nop 0\n\tbuffer_load_dwordx4 %1, %2, %3 offen lds" ::"s"(ldsaddr), "v"(voff), "s"(rs), "s"(soff) : "memory");
}
__device__ __forceinline__ void dma16sc(rsrc_t rs, unsigned ldsaddr, unsigned voff, unsigned soff) {
  asm volatile("s_mov_b32 m0, %0\n\ts_nop 0\n\tbuffer_load_dwordx4 %1, %2, %3 offen sc1 lds" ::"s"(ldsaddr), "v"(voff), "s"(rs), "s"(soff) : "memory");
}
__device__ __forceinline__ void ldv(u32x4& d, rsrc_t rs, unsigned voff, unsigned soff) { asm volatile("buffer_load_dwordx4 %0, %1, %2, %3 offen" : "+v"(d) : "v"(voff), "s"(rs), "s"(soff) : "memory"); }
__device__ __forceinline__ s16x4 trd(const LAS char* p) {
  typedef short v4i16_t __attribute__((ext_vector_type(4)));
  return __builtin_bit_cast(s16x4, __builtin_amdgcn_ds_read_tr16_b64_v4i16((LAS v4i16_t*)p));
}
__device__ __forceinline__ bf16x8 cat(s16x4 a, s16x4 b) { return (bf16x8){a[0], a[1], a[2], a[3], b[0], b[1], b[2], b[3]}; }
__device__ __forceinline__ float dot2(unsigned a, unsigned b, float c) { return __builtin_amdgcn_fdot2_f32_bf16(__builtin_bit_cast(bf16x2, a), __builtin_bit_cast(bf16x2, b), c, false); }
__device__ __forceinline__ float swap32_add(float v) { auto r = __builtin_amdgcn_permlane32_swap(__float_as_uint(v), __float_as_uint(v), false, false); return __uint_as_float(r[0]) + __uint_as_float(r[1]); }
__device__ __forceinline__ float swap32_max(float v) { auto r = __builtin_amdgcn_permlane32_swap(__float_as_uint(v), __float_as_uint(v), false, false); return __builtin_fmaxf(__uint_as_float(r[0]), __uint_as_float(r[1])); }
template <int N> __device__ __forceinline__ void wait_bar() { asm volatile("s_waitcnt vmcnt(%0)\n\ts_barrier" ::"n"(N) : "memory"); }
template <int N> __device__ __forceinline__ void wait_vm() { asm volatile("s_waitcnt vmcnt(%0)" ::"n"(N) : "memory"); }
__device__ __forceinline__ void lds_bar() { asm volatile("s_waitcnt lgkmcnt(0)\n\ts_barrier" ::: "memory"); }
__device__ __forceinline__ float wave_sum(float v) {
  v += __builtin_bit_cast(float, __builtin_amdgcn_update_dpp(0, __builtin_bit_cast(int, v), 0x111, 0xf, 0xf, true));
  v += __builtin_bit_cast(float, __builtin_amdgcn_update_dpp(0, __builtin_bit_cast(int, v), 0x112, 0xf, 0xf, true));
  v += __builtin_bit_cast(float, __builtin_amdgcn_update_dpp(0, __builtin_bit_cast(int, v), 0x114, 0xf, 0xf, true));
  v += __builtin_bit_cast(float, __builtin_amdgcn_update_dpp(0, __builtin_bit_cast(int, v), 0x118, 0xf, 0xf, true));
  v += __builtin_bit_cast(float, __builtin_amdgcn_update_dpp(0, __builtin_bit_cast(int, v), 0x142, 0xa, 0xf, true));
  v += __builtin_bit_cast(float, __builtin_amdgcn_update_dpp(0, __builtin_bit_cast(int, v), 0x143, 0xc, 0xf, true));
  return __builtin_bit_cast(float, __builtin_amdgcn_readlane(__builtin_bit_cast(int, v), 63));
}
constexpr int AUX_SC1 = 16;
__device__ __forceinline__ void st16sc(rsrc_t rs, u32x4 v, unsigned off) { __builtin_amdgcn_raw_buffer_store_b128(v, rs, off, 0, AUX_SC1); }
__device__ __forceinline__ void st8sc(rsrc_t rs, u32x2 v, unsigned off) { __builtin_amdgcn_raw_buffer_store_b64(v, rs, off, 0, AUX_SC1); }
__device__ __forceinline__ u32x4 ld16sc(rsrc_t rs, unsigned voff, unsigned soff) { return __builtin_amdgcn_raw_buffer_load_b128(rs, voff, soff, AUX_SC1); }
__device__ __forceinline__ u32x2 ld8sc(rsrc_t rs, unsigned voff) { return __builtin_amdgcn_raw_buffer_load_b64(rs, voff, 0, AUX_SC1); }
constexpr unsigned kSpinCap = 1u << 18;
struct Tag { unsigned lo, hi; };
__device__ __forceinline__ void wait_flags(rsrc_t rsW, unsigned off, Tag tag) {
#pragma clang loop unroll(disable)
  for (unsigned spins = 0;; ++spins) {
    const u32x2 v = ld8sc(rsW, off);
    if (__all(v[0] == tag.lo && v[1] == tag.hi)) break;
    if (spins > kSpinCap) { st8sc(rsW, (u32x2){1u, off}, WS_TMO); break; }
    __builtin_amdgcn_s_sleep(2);
  }
  __builtin_amdgcn_fence(__ATOMIC_ACQUIRE, "wavefront");
}
__device__ __forceinline__ u32x4 check_granule(u32x4 g, rsrc_t rsW, unsigned off, Tag tag) {
  if (!__all(g[2] == tag.lo && g[3] == tag.hi)) {
#pragma clang loop unroll(disable)
    for (unsigned spins = 0;; ++spins) {
      g = ld16sc(rsW, off, 0);
      if (__all(g[2] == tag.lo && g[3] == tag.hi)) break;
      if (spins > kSpinCap) { st8sc(rsW, (u32x2){2u, off}, WS_TMO); break; }
      __builtin_amdgcn_s_sleep(2);
    }
  }
  return g;
}

template <int CLS> constexpr int nld() { return CLS == 0 ? 4 : 3; }
enum { OP_GA = 100, OP_GB = 101, OP_OTHER = 102 };
template <int CLS> struct Sched {
  int id[256]; int n; int bar_at[17];
  constexpr void put(int what, int cnt) { for (int i = 0; i < cnt; ++i) id[n++] = what; }
  constexpr Sched() : id{}, n(0), bar_at{} {
    const int nl = nld<CLS>();
    put(0, nl); put(1, nl); put(2, nl);
    bar_at[0] = n;
    put(3, nl); put(4, nl);
    for (int k = 1; k <= 16; ++k) {
      bar_at[k] = n;
      if (k == 4) put(OP_GA, CLS == 0 ? 1 : 2);
      if (k == 12) put(OP_GB, 2);
      if (k == 8) put(OP_OTHER, CLS == 0 ? 4 : 2);
      if (k + 4 < 16) put(k + 4, nl);
      else if (k == 12 || k == 15) put(OP_OTHER, 2); else if (k == 13 || k == 14) put(OP_OTHER, 4);
    }
  }
  constexpr int younger(int what, int k) const { int last = -1; for (int i = 0; i < bar_at[k]; ++i) if (id[i] == what) last = i; return bar_at[k] - 1 - last; }
};
template <int CLS> constexpr int waitN(int k) {
  constexpr Sched<CLS> S{};
  int n = 63;
  if (k < 16) n = S.younger(k, k);
  if (k == 7) { const int g = S.younger(OP_GA, k); n = g < n ? g : n; }
  if (k == 15) { const int g = S.younger(OP_GB, k); n = g < n ? g : n; }
  if (k == 16) n = 6;
  return n > 63 ? 63 : n;
}
template <int N> __device__ __forceinline__ void bar_n() { asm volatile("s_waitcnt vmcnt(%0) lgkmcnt(0)\n\ts_barrier" ::"n"(N) : "memory"); }
constexpr int crow(int s) { return (s + 2) & 7; }

struct Ctx {
  Params p; rsrc_t rsW; Tag tag; unsigned lds0; const LAS char* L; int b, w8, lane;
};

template <int CLS>
__device__ __forceinline__ void ab_wave(Ctx& X, const int pnl, const int c, const int w, const int tt_c, const int ct_c, u32x4 (&kvq)[5] ST_ARG) {
  const Params& p = X.p; const unsigned lds0 = X.lds0; const LAS char* L = X.L; const int lane = X.lane; const rsrc_t rsW = X.rsW;
  const int l31 = lane & 31, hh = lane >> 5, blk = (lane >> 4) & 1, q4 = (lane & 15) >> 2, pp = lane & 3;
  const int r0 = (pnl + 2 * (c & 3)) & 7;
  rsrc_t rsA, rsB[4];
  unsigned voA[4], voB[4], soA, ldA[4], ldB[4];
  if constexpr (CLS == 0) {
    rsA = mkrs(p.x + (size_t)pnl * 128 * D_, (S_ - pnl * 128) * D_ * 2);
#pragma unroll
    for (int j = 0; j < 4; ++j) { const int par = j & 1; voA[j] = (8 * par + (lane >> 3)) * 1024 + (((lane & 7) ^ ((4 * par + (lane >> 4)) & 7)) << 4) + (j >> 1) * 16384; ldA[j] = (4 * w + j) * 1024; }
    soA = w * 32 * 1024;
  } else {
    rsA = mkrs(p.wqkv + c * 96, (D_ * 3 * D_ - c * 96) * 2);
#pragma unroll
    for (int j = 0; j < 3; ++j) { const int o = (3 * w + j) * 1024 + lane * 16, row = o / 192, ch = (o % 192) >> 4; voA[j] = row * 3072 + ch * 16; ldA[j] = 16384 + (3 * w + j) * 1024; }
    soA = 0;
  }
  auto issueA = [&](auto sc, auto jc) {
    constexpr int s = decltype(sc)::value, j = decltype(jc)::value; constexpr int buf = (s % NSLOT) * SLOT;
    dma16(rsA, lds0 + buf + ldA[j], voA[j], soA + ((s + r0) & 7) * (CLS == 0 ? 128 : 64 * 3072));
  };
  const rsrc_t rsX = mkrs(p.x, S_ * D_ * 2);
  STAMP(21);
  dma16(rsX, lds0 + L_G + X.w8 * 1024, lane * 16, (X.b * 8 + X.w8) * 1024);
  static_for<0, 3>([&](auto sc) { static_for<0, nld<CLS>()>([&](auto jc) { issueA(sc, jc); }); });
  STAMP(22);
  {
    u64 epoch; asm volatile("s_load_dwordx2 %0, %1, 0x0\n\ts_waitcnt lgkmcnt(0)" : "=s"(epoch) : "s"(p.ws + WS_EPOCH) : "memory");
    epoch += 1; X.tag.lo = (unsigned)epoch; X.tag.hi = (unsigned)(epoch >> 32);
  }
#pragma unroll
  for (int j = 0; j < nld<CLS>(); ++j) {
    const int i = CLS == 0 ? 4 * w + j : 16 + 3 * w + j;
    const bool isg = i < 14; const int ii = isg ? i : i - 14;
    const int o = ii * 1024 + lane * 16, row = o / 224, ch = (o % 224) >> 4;
    voB[j] = row * 3584 + ch * 16; ldB[j] = isg ? ii * 1024 : UOFF + ii * 1024;
    rsB[j] = mkrs((isg ? p.wg : p.wu) + c * 112, (D_ * F_ - c * 112) * 2);
  }
  const rsrc_t rsD = mkrs(p.wd + ct_c * 64, (F_ * D_ - ct_c * 64) * 2);
  const unsigned voC = (lane >> 3) * 1024 + (((lane & 7) ^ ((((lane >> 3) >> 1) & 1) << 2)) << 4);
  auto issueC = [&](auto sc) {
    constexpr int s = decltype(sc)::value;
    dma16(rsD, lds0 + crow(s) * 16384 + X.w8 * 2048, voC, (14 * X.w8 + s) * 16384);
    dma16(rsD, lds0 + crow(s) * 16384 + X.w8 * 2048 + 1024, voC, (14 * X.w8 + s) * 16384 + 8192);
  };
  u32x4 dummy = {0, 0, 0, 0};
  auto issue = [&](auto sc, auto jc) {
    constexpr int s = decltype(sc)::value, j = decltype(jc)::value; constexpr int buf = (s % NSLOT) * SLOT;
    if constexpr (s < 8) issueA(sc, jc);
    else dma16(rsB[j], lds0 + buf + ldB[j], voB[j], ((s + r0) & 7) * (64 * 3584));
  };
  {
    STAMP(23);
    wait_vm<3 * nld<CLS>()>();
    STAMP(24);
    const u32x4 v = *(const LAS u32x4*)(L + L_G + X.w8 * 1024 + lane * 16);
    float e[8];
#pragma unroll
    for (int i = 0; i < 4; ++i) { e[2 * i] = bf2f(v[i] & 0xffffu); e[2 * i + 1] = __uint_as_float(v[i] & 0xffff0000u); }
    const float mean = wave_sum(((e[0] + e[1]) + (e[2] + e[3])) + ((e[4] + e[5]) + (e[6] + e[7]))) * (1.0f / D_);
    float q = 0;
#pragma unroll
    for (int i = 0; i < 8; ++i) { const float d = e[i] - mean; q += d * d; }
    q = wave_sum(q);
    const unsigned mb = f2bf(mean), sb = f2bf(sqrtf(rbf(rbf(q * (1.0f / D_)) + 1e-5f)));
    if (lane == 0) st16sc(rsW, (u32x4){mb | (sb << 16), 0u, X.tag.lo, X.tag.hi}, WS_STATS + (X.b * 8 + X.w8) * 16);
  }
  const int tt = w;
  const int xrow = tt * 32 + l31;
  const unsigned xad = xrow * 128 + ((hh ^ ((xrow >> 1) & 7)) << 4);
  constexpr int NTA = CLS == 0 ? 2 : 1, NTB = CLS == 0 ? 4 : 3;
  const unsigned adA = 16384 + (8 * hh + q4) * 192 + blk * 32 + pp * 8 + (CLS == 0 ? 0 : 128);
  const unsigned adB = blk * UOFF + (8 * hh + q4) * 224 + pp * 8 + (CLS == 0 ? 0 : 4 * 32);
  bf16x8 xf[32];
  f32x16 accA[NTA], accB[NTB];
#pragma unroll
  for (int j = 0; j < NTA; ++j) accA[j] = f32x16{};
#pragma unroll
  for (int j = 0; j < NTB; ++j) accB[j] = f32x16{};
  float cs = 0.f;
  const unsigned ONES = 0x3F803F80u;
  const bool pubA = w < NTA, pubB = w < NTB;
  float rinv = 0.f; unsigned nmb = 0;
  STAMP(1);
  bf16x8 wa[3][NTA], wb[2][NTB];
  unsigned curW, curx;
  auto setbase = [&](auto sc) {
    constexpr int s = decltype(sc)::value; constexpr int buf = (s % NSLOT) * SLOT;
    if constexpr (s < 8) { curW = adA + buf; asm volatile("" : "+v"(curW)); curx = xad + buf; asm volatile("" : "+v"(curx)); }
    else { curW = adB + buf; asm volatile("" : "+v"(curW)); }
  };
  auto fetch = [&](auto gc) {
    constexpr int g = decltype(gc)::value; constexpr int ks = g & 3;
    if constexpr (g < 32) {
      xf[g] = *(const LAS bf16x8*)(L + (curx ^ (ks << 5)));
#pragma unroll
      for (int j = 0; j < NTA; ++j) wa[g % 3][j] = cat(trd(L + curW + j * 64 + (16 * ks) * 192), trd(L + curW + j * 64 + (16 * ks + 4) * 192));
    } else {
#pragma unroll
      for (int j = 0; j < NTB; ++j) wb[g & 1][j] = cat(trd(L + curW + j * 32 + (16 * ks) * 224), trd(L + curW + j * 32 + (16 * ks + 4) * 224));
    }
  };
  auto colsum = [&](auto gc, auto ntc) {
    constexpr int g = decltype(gc)::value, NT = decltype(ntc)::value;
    static_for<0, NT>([&](auto jc) { constexpr int j = decltype(jc)::value;
      if (w == j) { u32x4 u; if constexpr (g < 32) u = __builtin_bit_cast(u32x4, wa[g % 3][j < NTA ? j : 0]); else u = __builtin_bit_cast(u32x4, wb[g & 1][j]); cs = dot2(u[0], ONES, cs); cs = dot2(u[1], ONES, cs); cs = dot2(u[2], ONES, cs); cs = dot2(u[3], ONES, cs); } });
  };
  auto publish_cs = [&](unsigned base, int tile) {
    const float cf = swap32_add(cs); cs = 0.f;
    const float nb = __shfl_down(cf, 1);
    if (hh == 0 && (lane & 1) == 0) st16sc(rsW, (u32x4){__float_as_uint(cf), __float_as_uint(nb), X.tag.lo, X.tag.hi}, base + tile * 256 + l31 * 8);
  };
  auto sum_cs = [&](unsigned gbase, int pstride, int ncol, int tile0) {
    const int t = X.w8 * 64 + lane;
    if (X.w8 * 64 < ncol) {
      const int tc = t < ncol ? t : ncol - 1;
      float sum = 0.f; bool ok = true;
#pragma unroll
      for (int part = 0; part < 8; ++part) {
        const u32x4 g = *(const LAS u32x4*)(L + L_G + part * pstride + (tc >> 1) * 16);
        ok = ok && g[2] == X.tag.lo && g[3] == X.tag.hi;
        sum += __uint_as_float((tc & 1) ? g[1] : g[0]);
      }
      if (!__all(ok)) {
        sum = 0.f;
#pragma clang loop unroll(disable)
        for (int part = 0; part < 8; ++part) {
          const u32x4 g = check_granule((u32x4){0u, 0u, ~X.tag.lo, 0u}, rsW, gbase + part * pstride + (tc >> 1) * 16, X.tag);
          sum += __uint_as_float((tc & 1) ? g[1] : g[0]);
        }
      }
      if (t < ncol) *(LAS float*)(L + L_CS + tile0 * 128 + t * 4) = sum;
    }
  };
  auto csfrag = [&](int tile) {
    const float cv = *(const LAS float*)(L + L_CS + tile * 128 + l31 * 4);
    const unsigned h0 = f2bf(cv); const float r1 = cv - bf2f(h0); const unsigned h1 = f2bf(r1); const unsigned h2 = f2bf(r1 - bf2f(h1));
    return hh == 0 ? (bf16x8){(short)h0, (short)h1, (short)h2, 0, 0, 0, 0, 0} : (bf16x8){0, 0, 0, 0, 0, 0, 0, 0};
  };
  const unsigned csA = WS_CSA + c * 8192, csB = WS_CSB + c * 16384;
  unsigned fa_off;
  { const int j = lane < 10 ? lane : 0; const int typ = j < 4 ? 1 : (j < 8 ? 2 : 0);
    int pq = j < 8 ? 2 * ct_c - 2 + (j & 3) : 2 * ct_c + (j & 1); pq = pq < 0 ? pq + 2 : pq;
    fa_off = WS_FLAGA + (pq * 16 + (16 * typ + (tt_c >> 1)) / 3) * 8; }
  auto after_bar = [&](auto nc) {
    constexpr int n = decltype(nc)::value;
    if constexpr (n == 4) {
      dma16sc(rsW, lds0 + L_G + X.w8 * 1024, lane * 16, csA + X.w8 * 1024);
      if constexpr (CLS == 1) dma16sc(rsW, lds0 + L_ST + (w & 1) * 1024, lane * 16, WS_STATS + pnl * 2048 + (w & 1) * 1024);
    }
    if constexpr (n == 12) {
      if (X.w8 == 0 && lane == 0) st8sc(rsW, (u32x2){X.tag.lo, X.tag.hi}, WS_FLAGA + (pnl * 16 + c) * 8);
      dma16sc(rsW, lds0 + L_G + X.w8 * 2048, lane * 16, csB + X.w8 * 2048); dma16sc(rsW, lds0 + L_G + X.w8 * 2048 + 1024, lane * 16, csB + X.w8 * 2048 + 1024);
    }
    if constexpr (n == 14) { if (X.w8 == 0) dma16sc(rsW, lds0 + L_ST, fa_off, 0); }
    if constexpr (n == 7) sum_cs(csA, 1024, 96, 0);
    if constexpr (n == 15) sum_cs(csB, 2048, 224, 3);
  };
  auto refill = [&](auto nc, auto pc) {
    constexpr int n = decltype(nc)::value, pos = decltype(pc)::value;
    if constexpr (n + 4 < 16) { if constexpr (CLS == 0) issue(ic<n + 4>{}, pc); else if constexpr (pos >= 1) issue(ic<n + 4>{}, ic<pos - 1>{}); }
    else if constexpr (n == 12) { if constexpr (pos == 1) issueC(ic<0>{}); }
    else if constexpr (n == 13) { if constexpr (pos == 1) issueC(ic<1>{}); if constexpr (pos == 3) issueC(ic<2>{}); }
    else if constexpr (n == 14) { if constexpr (pos == 1) issueC(ic<3>{}); if constexpr (pos == 3) issueC(ic<4>{}); }
    else if constexpr (n == 15) { if constexpr (pos == 1) issueC(ic<5>{}); }
  };
  bar_n<waitN<CLS>(0)>();
  STAMP(25);
  static_for<3, 5>([&](auto sc) { static_for<0, nld<CLS>()>([&](auto jc) { issueA(sc, jc); }); });
  setbase(ic<0>{}); fetch(ic<0>{}); fetch(ic<1>{});
  static_for<0, 16>([&](auto sc) {
    constexpr int s = decltype(sc)::value;
    static_for<0, 4>([&](auto kc) {
      constexpr int ks = decltype(kc)::value, g = 4 * s + ks;
      constexpr int bk = s + 1 < 8 ? 2 : 3, pbk = s < 8 ? 2 : 3;
      if constexpr (ks == bk) { bar_n<waitN<CLS>(s + 1)>(); after_bar(ic<s + 1>{}); if constexpr (s + 1 < 16) setbase(ic<s + 1>{}); }
      if constexpr (ks == bk && s == 15) {
        const u32x2 fl = *(const LAS u32x2*)(L + L_ST + lane * 16);
        if (!__all(fl[0] == X.tag.lo && fl[1] == X.tag.hi)) wait_flags(rsW, fa_off, X.tag);
#pragma unroll
        for (int j = 0; j < 2; ++j) {
          int kt = 8 * ct_c - 8 + 2 * X.w8 + j; kt = kt < 0 ? 0 : kt;
          kvq[j] = ld16sc(rsW, lane * 16, WS_K + (tt_c * 64 + kt) * 1024);
          kvq[2 + j] = ld16sc(rsW, lane * 16, WS_V + (tt_c * 64 + kt) * 1024);
        }
        kvq[4] = ld16sc(rsW, lane * 16, WS_Q + (tt_c * 64 + 8 * ct_c + X.w8) * 1024);
      }
      if constexpr (ks >= bk) refill(ic<s + 1>{}, ic<ks - bk>{});
      else if constexpr (s > 0 && ks + 4 - pbk < 4) refill(ic<s>{}, ic<ks + 4 - pbk>{});
      SB0();
      if constexpr (s == 0) { colsum(ic<g>{}, ic<NTA>{}); SB0(); }
      if constexpr (s == 8) { colsum(ic<g>{}, ic<NTB>{}); SB0(); }
      if constexpr (g + 2 < 32) fetch(ic<g + 2>{});
      if constexpr (g + 1 >= 32 && g + 1 < 64) fetch(ic<g + 1>{});
      if constexpr (g < 32) {
#pragma unroll
        for (int j = 0; j < NTA; ++j) accA[j] = MFMA(wa[g % 3][j], xf[g], accA[j]);
        if constexpr (g < 30) { for (int j = 0; j < NTA; ++j) { SGB(0x8, 1); SGB(0x100, 3); } }
      } else {
#pragma unroll
        for (int j = 0; j < NTB; ++j) accB[j] = MFMA(wb[g & 1][j], xf[g - 32], accB[j]);
        if constexpr (ks == 2) { SGB(0x8, 1); SGB(0x100, NTB); SGB(0x8, 1); SGB(0x100, NTB); SGB(0x8, NTB - 2); }
        else { for (int j = 0; j < NTB; ++j) { SGB(0x8, 1); SGB(0x100, 2); } }
      }
      SB0();
    });
    if constexpr (s == 0) { if (pubA) publish_cs(csA + r0 * 1024, (CLS == 0 ? 0 : 2) + w); }
    if constexpr (s == 8) { if (pubB) publish_cs(csB + r0 * 2048, (CLS == 0 ? 0 : 4) + w); }
    if constexpr (s == 7) {
      STAMP(2);
      u32x4 stg = *(const LAS u32x4*)(L + L_ST + (tt * 32 + l31) * 16);
      stg = check_granule(stg, rsW, WS_STATS + (pnl * 128 + tt * 32 + l31) * 16, X.tag);
      rinv = 1.0f / __uint_as_float(stg[0] & 0xffff0000u);
      nmb = (stg[0] & 0xffffu) ^ 0x8000u;
      const bf16x8 mfrag = hh == 0 ? (bf16x8){(short)nmb, (short)nmb, (short)nmb, 0, 0, 0, 0, 0} : (bf16x8){0, 0, 0, 0, 0, 0, 0, 0};
      const int gt = pnl * 4 + tt;
#pragma unroll
      for (int j = 0; j < NTA; ++j) {
        const int tl = (CLS == 0 ? 0 : 2) + j;
        accA[j] = MFMA(csfrag(tl), mfrag, accA[j]);
        const int T = 3 * c + tl;
        const float sc_ = T < 16 ? rinv * (0.25f * 1.4426950408889634f) : rinv;
#pragma unroll
        for (int hd = 0; hd < 2; ++hd) {
          u32x4 o;
#pragma unroll
          for (int i = 0; i < 4; ++i) o[i] = pk2(accA[j][8 * hd + 2 * i] * sc_, accA[j][8 * hd + 2 * i + 1] * sc_);
          st16sc(rsW, o, WS_Q + ((2 * T + hd) * 64 + gt) * 1024 + lane * 16);
        }
      }
      STAMP(3);
    }
  });
  STAMP(4);
  {
    const bf16x8 mfrag = hh == 0 ? (bf16x8){(short)nmb, (short)nmb, (short)nmb, 0, 0, 0, 0, 0} : (bf16x8){0, 0, 0, 0, 0, 0, 0, 0};
    const int gt = pnl * 4 + tt;
    const float r2 = rinv * rinv;
#pragma unroll
    for (int j = 0; j < NTB; ++j) {
      const int tl = (CLS == 0 ? 0 : 4) + j;
      accB[j] = MFMA(csfrag(3 + tl), mfrag, accB[j]);
      u32x4 o;
#pragma unroll
      for (int i = 0; i < 4; ++i) o[i] = pk2(accB[j][2 * i] * accB[j][2 * i + 8] * r2, accB[j][2 * i + 1] * accB[j][2 * i + 9] * r2);
      st16sc(rsW, o, WS_ACT + (gt * 144 + 7 * c + tl) * 1024 + lane * 16);
    }
  }
  STAMP(5);
}

__device__ __forceinline__ void attn_c_wave(const Ctx& X, const int pnl, const int c, const int hd, const int qb, u32x4 (&kvq)[5] ST_ARG) {
  const Params& p = X.p; const unsigned lds0 = X.lds0; const LAS char* L = X.L; const rsrc_t rsW = X.rsW; const int lane = X.lane, w = X.w8;
  const int l31 = lane & 31, hh = lane >> 5;
  const int tt = hd, ct = qb;
  const u32x2 tagv = {X.tag.lo, X.tag.hi};
  if (w == 0) *(LAS u32x4*)(L + L_G + lane * 16) = (u32x4){0x3F803F80u, 0x3F803F80u, 0x3F803F80u, 0x3F803F80u};
#pragma unroll
  for (int j = 0; j < 2; ++j) {
    *(LAS u32x4*)(L + LK + (2 * w + j) * 1024 + lane * 16) = kvq[j];
    *(LAS u32x4*)(L + LV + (2 * w + j) * 1024 + lane * 16) = kvq[2 + j];
  }
  const bf16x8 qf = __builtin_bit_cast(bf16x8, kvq[4]);
  lds_bar();
  STAMP(6);
  const int qt = 8 * qb + w;
  const int first = qb == 0 ? 8 - w : 0;
  const f32x16 zero = f32x16{};
  const float NEG = -1e30f;
  const LAS char* kp = L + LK + w * 1024 + lane * 16;
  auto kfr = [&](int i) { return *(const LAS bf16x8*)(kp + i * 1024); };
  const int tq = l31 - 4 * hh;
  const int blk = (lane >> 4) & 1, q4 = (lane & 15) >> 2, pp = lane & 3;
  const unsigned vl = blk ? (unsigned)L_G : (unsigned)(LV + w * 1024 + (pp & 1) * 512 + (4 * hh + q4) * 16 + (pp >> 1) * 8);
  const unsigned vs = blk ? 0u : 1024u;
  u32x4 act[18][2];
  auto load_act = [&](auto lo, auto hi) {
    static_for<decltype(lo)::value, decltype(hi)::value>([&](auto sc) {
      constexpr int s = decltype(sc)::value;
      const int ks = s < 14 ? 14 * w + s : 112 + 4 * w + (s - 14);
#pragma unroll
      for (int tk = 0; tk < 2; ++tk) act[s][tk] = ld16sc(rsW, lane * 16, WS_ACT + ((2 * tt + tk) * 144 + ks) * 1024);
    });
  };
  f32x16 o = zero;
  static constexpr int ORD[9] = {0, 8, 7, 6, 5, 4, 3, 2, 1};
  const int ntile = 9 - (first > 1 ? first - 1 : 0);
  float mx;
  {
    float a = NEG, b2 = NEG;
    f32x16 s[3]; bf16x8 kf[3];
    kf[0] = kfr(0); kf[1] = kfr(8); kf[2] = kfr(7);
    s[0] = MFMA(kf[0], qf, zero); s[1] = MFMA(kf[1], qf, zero);
    SB0();
    static_for<2, 10>([&](auto jc) {
      constexpr int j = decltype(jc)::value;
      if constexpr (j == 4) {
        wait_vm<0>();
        if (lane == 0) st8sc(rsW, tagv, WS_FLAGB + (((pnl * 4 + (w & 3)) * 16 + c) * 2 + (w >> 2)) * 8);
        STAMP(12);
      }
      if (j <= ntile) {
        if constexpr (j < 9) s[j % 3] = MFMA(kf[j % 3], qf, zero);
        if constexpr (j + 1 < 9) kf[(j + 1) % 3] = kfr(ORD[j + 1]);
        SB0();
        if constexpr (j == 2) {
#pragma unroll
          for (int r = 0; r < 16; r += 2) {
            const float v0 = ((r & 3) + 8 * (r >> 2) > tq) ? (first > 0 ? NEG : s[0][r]) : s[1][r];
            const float v1 = (((r + 1) & 3) + 8 * (r >> 2) > tq) ? (first > 0 ? NEG : s[0][r + 1]) : s[1][r + 1];
            if (r & 2) b2 = __builtin_fmaxf(__builtin_fmaxf(b2, v0), v1); else a = __builtin_fmaxf(__builtin_fmaxf(a, v0), v1);
          }
        } else {
#pragma unroll
          for (int r = 0; r < 16; r += 4) { a = __builtin_fmaxf(__builtin_fmaxf(a, s[(j - 1) % 3][r]), s[(j - 1) % 3][r + 1]); b2 = __builtin_fmaxf(__builtin_fmaxf(b2, s[(j - 1) % 3][r + 2]), s[(j - 1) % 3][r + 3]); }
        }
        SB0();
      }
    });
    mx = swap32_max(__builtin_fmaxf(a, b2));
  }
  STAMP(11);
  {
    f32x16 negm;
#pragma unroll
    for (int r = 0; r < 16; ++r) negm[r] = -mx;
    f32x16 s[3]; bf16x8 kf[3];
    auto vfr = [&](int i, int st) { const LAS char* vp = L + (vl + i * vs) + st * 256; return cat(trd(vp), trd(vp + 128)); };
    kf[0] = kfr(0); kf[1] = kfr(8); kf[2] = kfr(7);
    s[0] = MFMA(kf[0], qf, negm); s[1] = MFMA(kf[1], qf, negm);
    SB0();
    static_for<2, 10>([&](auto jc) {
      constexpr int j = decltype(jc)::value;
      if (j <= ntile) {
        if constexpr (j < 9) s[j % 3] = MFMA(kf[j % 3], qf, negm);
        if constexpr (j + 1 < 9) kf[(j + 1) % 3] = kfr(ORD[j + 1]);
        if constexpr (j == 2) {
          const bf16x8 va0 = vfr(0, 0), va1 = vfr(0, 1), vb0 = vfr(8, 0), vb1 = vfr(8, 1);
          SB0();
          u32x4 a0, a1, b0, b1;
#pragma unroll
          for (int i = 0; i < 8; ++i) {
            const int r = 2 * i, k0 = (r & 3) + 8 * (r >> 2);
            const bool m0 = k0 > tq, m1 = k0 + 1 > tq;
            const float v0 = m0 ? (first > 0 ? NEG : s[0][r]) : s[1][r], v1 = m1 ? (first > 0 ? NEG : s[0][r + 1]) : s[1][r + 1];
            const unsigned pk = pk2(__builtin_amdgcn_exp2f(v0), __builtin_amdgcn_exp2f(v1));
            const unsigned msk = m0 ? 0xffffffffu : (m1 ? 0xffff0000u : 0u);
            if (i < 4) { a0[i] = pk & msk; b0[i] = pk & ~msk; } else { a1[i - 4] = pk & msk; b1[i - 4] = pk & ~msk; }
          }
          o = MFMA(va0, __builtin_bit_cast(bf16x8, a0), o); o = MFMA(vb0, __builtin_bit_cast(bf16x8, b0), o);
          o = MFMA(va1, __builtin_bit_cast(bf16x8, a1), o); o = MFMA(vb1, __builtin_bit_cast(bf16x8, b1), o);
        } else {
          constexpr int i = ORD[j - 1];
          const bf16x8 v0 = vfr(i, 0), v1 = vfr(i, 1);
          SB0();
          f32x16& t = s[(j - 1) % 3];
          u32x4 p0, p1;
#pragma unroll
          for (int r = 0; r < 8; ++r) t[r] = __builtin_amdgcn_exp2f(t[r]);
#pragma unroll
          for (int r = 0; r < 4; ++r) p0[r] = pk2(t[2 * r], t[2 * r + 1]);
#pragma unroll
          for (int r = 8; r < 10; ++r) t[r] = __builtin_amdgcn_exp2f(t[r]);
          o = MFMA(v0, __builtin_bit_cast(bf16x8, p0), o);
          SB0();
#pragma unroll
          for (int r = 10; r < 16; ++r) t[r] = __builtin_amdgcn_exp2f(t[r]);
#pragma unroll
          for (int r = 0; r < 4; ++r) p1[r] = pk2(t[8 + 2 * r], t[9 + 2 * r]);
          o = MFMA(v1, __builtin_bit_cast(bf16x8, p1), o);
        }
        SB0();
      }
    });
  }
  STAMP(16);
  {
    const float li = 1.0f / o[8];
    u32x4 ov;
#pragma unroll
    for (int i = 0; i < 4; ++i) ov[i] = pk2(o[2 * i] * li, o[2 * i + 1] * li);
    st16sc(rsW, ov, WS_ACT + (qt * 144 + 112 + hd) * 1024 + lane * 16);
  }
  dma16sc(rsW, lds0 + L_G + 1024 + w * 1024, lane * 16, WS_FLAGB + tt * 512);
  wait_vm<0>();
  if (lane == 0) st8sc(rsW, tagv, WS_FLAGO + (qt * 32 + hd) * 8);
  STAMP(13);
  {
    const u32x2 fl = *(const LAS u32x2*)(L + L_G + 1024 + w * 1024 + lane * 8);
    if (!__all(fl[0] == X.tag.lo && fl[1] == X.tag.hi)) wait_flags(rsW, WS_FLAGB + tt * 512 + lane * 8, X.tag);
  }
  STAMP(14);
  load_act(ic<0>{}, ic<4>{});
  lds_bar();
  STAMP(7);
  const rsrc_t rsD = mkrs(p.wd + ct * 64, (F_ * D_ - ct * 64) * 2), rsO = mkrs(p.wo + ct * 64, (D_ * D_ - ct * 64) * 2);
  const unsigned vo = (lane >> 3) * 1024 + (((lane & 7) ^ ((((lane >> 3) >> 1) & 1) << 2)) << 4);
  auto issueW = [&](auto sc) {
    constexpr int s = decltype(sc)::value;
    const unsigned d = lds0 + crow(s) * 16384 + w * 2048;
    if constexpr (s < 14) { dma16(rsD, d, vo, (14 * w + s) * 16384); dma16(rsD, d + 1024, vo, (14 * w + s) * 16384 + 8192); }
    else { dma16(rsO, d, vo, (4 * w + s - 14) * 16384); dma16(rsO, d + 1024, vo, (4 * w + s - 14) * 16384 + 8192); }
  };
  issueW(ic<6>{}); issueW(ic<7>{});
  load_act(ic<4>{}, ic<14>{});
  const int tid = w * 64 + lane;
  const unsigned ra0 = w * 2048 + (4 * hh + q4) * 128 + (((2 * blk + (pp >> 1)) ^ (((q4 >> 1) & 1) << 2)) << 4) + (pp & 1) * 8;
  unsigned rb[2][2];
  rb[0][0] = ra0; rb[1][0] = ra0 ^ 64; rb[0][1] = ra0 + 65536; rb[1][1] = (ra0 ^ 64) + 65536;
  f32x16 acc[2][2];
#pragma unroll
  for (int i = 0; i < 2; ++i)
#pragma unroll
    for (int j = 0; j < 2; ++j) acc[i][j] = f32x16{};
  u32x4 xres;
  STAMP(8);
  static_for<0, 18>([&](auto sc) {
    constexpr int s = decltype(sc)::value;
    if constexpr (s == 6) wait_vm<2 + 20 + 12>();
    if constexpr (s == 7) wait_vm<20 + 14>();
    if constexpr (s == 7) STAMP(18);
    if constexpr (s == 11) STAMP(19);
    if constexpr (s == 12) STAMP(20);
    if constexpr (s == 11) {
      wait_flags(rsW, WS_FLAGO + tt * 512 + lane * 8, X.tag);
      load_act(ic<14>{}, ic<18>{});
      xres = *(const u32x4*)(p.x + (size_t)(tt * 64 + (tid >> 3)) * D_ + ct * 64 + (tid & 7) * 8);
      if (X.b == 0 && w == 7) {
#pragma clang loop unroll(disable)
        for (int j = 0; j < 4; ++j) wait_flags(rsW, WS_FLAGA + (j * 64 + lane) * 8, X.tag);
        if (lane == 0) st8sc(rsW, tagv, WS_EPOCH);
      }
    }
    if constexpr (s >= 8 && s < 11) { constexpr int last = s - 1 < 9 ? s - 1 : 9; wait_vm<(last - (s - 8)) * 2>(); }
    constexpr int row = crow(s), hf = row >> 2, ro = (row & 3) * 16384;
    const bf16x8 w0 = cat(trd(L + rb[0][hf] + ro), trd(L + rb[0][hf] + ro + 1024));
    const bf16x8 w1 = cat(trd(L + rb[1][hf] + ro), trd(L + rb[1][hf] + ro + 1024));
    acc[0][0] = MFMA(w0, __builtin_bit_cast(bf16x8, act[s][0]), acc[0][0]);
    acc[0][1] = MFMA(w0, __builtin_bit_cast(bf16x8, act[s][1]), acc[0][1]);
    acc[1][0] = MFMA(w1, __builtin_bit_cast(bf16x8, act[s][0]), acc[1][0]);
    acc[1][1] = MFMA(w1, __builtin_bit_cast(bf16x8, act[s][1]), acc[1][1]);
    if constexpr (s + 8 < 18) { asm volatile("s_waitcnt lgkmcnt(0)" ::: "memory"); issueW(ic<s + 8>{}); }
  });
  STAMP(9);
  lds_bar();
#pragma unroll
  for (int nt = 0; nt < 2; ++nt)
#pragma unroll
    for (int tk = 0; tk < 2; ++tk)
#pragma unroll
      for (int g = 0; g < 4; ++g) {
        const int token = 32 * tk + l31, ch = 8 * nt + 2 * g + hh;
        *(LAS f32x4*)(L + w * 16384 + token * 256 + ((ch ^ (token & 15)) << 4)) = (f32x4){acc[nt][tk][4 * g], acc[nt][tk][4 * g + 1], acc[nt][tk][4 * g + 2], acc[nt][tk][4 * g + 3]};
      }
  lds_bar();
  {
    const int token = tid >> 3, c8 = tid & 7;
    f32x4 s0 = {0, 0, 0, 0}, s1 = {0, 0, 0, 0};
#pragma unroll
    for (int ww = 0; ww < 8; ++ww) {
      s0 += *(const LAS f32x4*)(L + ww * 16384 + token * 256 + (((2 * c8) ^ (token & 15)) << 4));
      s1 += *(const LAS f32x4*)(L + ww * 16384 + token * 256 + (((2 * c8 + 1) ^ (token & 15)) << 4));
    }
    u32x4 ow;
    ow[0] = pk2(s0[0] + bf2f(xres[0] & 0xffffu), s0[1] + __uint_as_float(xres[0] & 0xffff0000u));
    ow[1] = pk2(s0[2] + bf2f(xres[1] & 0xffffu), s0[3] + __uint_as_float(xres[1] & 0xffff0000u));
    ow[2] = pk2(s1[0] + bf2f(xres[2] & 0xffffu), s1[1] + __uint_as_float(xres[2] & 0xffff0000u));
    ow[3] = pk2(s1[2] + bf2f(xres[3] & 0xffffu), s1[3] + __uint_as_float(xres[3] & 0xffff0000u));
    *(u32x4*)(p.out + (size_t)(tt * 64 + token) * D_ + ct * 64 + c8 * 8) = ow;
  }
  STAMP(10);
}

__global__ __launch_bounds__(512, 2) void k_fused(Params p) {
  extern __shared__ __attribute__((aligned(16))) char smem[];
  STAMP_DECL; STAMP(0);
  Ctx X;
  X.p = p; X.b = blockIdx.x; X.w8 = rfl(threadIdx.x >> 6); X.lane = threadIdx.x & 63;
  X.lds0 = (unsigned)(size_t)smem; X.L = (const LAS char*)smem;
  X.rsW = mkrs(p.ws, WS_END);

  const int xcd = X.b & 7, r = X.b >> 3;
  const int pnl = (xcd >> 2) * 8 + (r & 7), c = (xcd & 3) * 4 + (r >> 3);
  const int u = xcd * 4 + (r & 3), v = r >> 2;
  u32x4 kvq[5];
  if (X.w8 < 4) ab_wave<0>(X, pnl, c, X.w8, u, v, kvq ST_PASS); else ab_wave<1>(X, pnl, c, X.w8 - 4, u, v, kvq ST_PASS);
  attn_c_wave(X, pnl, c, u, v, kvq ST_PASS);
  STAMP_FLUSH();
}

extern "C" void kernel_launch(void* const* d_in, const int* in_sizes, int n_in, void* d_out, int out_size, void* d_ws, size_t ws_size, hipStream_t stream) {
  Params p;
  p.x = (const u16*)d_in[0]; p.wqkv = (const u16*)d_in[1]; p.wo = (const u16*)d_in[2];
  p.wg = (const u16*)d_in[4]; p.wu = (const u16*)d_in[5]; p.wd = (const u16*)d_in[6];
  p.out = (u16*)d_out; p.ws = (char*)d_ws;
  (void)hipFuncSetAttribute((const void*)k_fused, hipFuncAttributeMaxDynamicSharedMemorySize, L_END);
  k_fused<<<256, 512, L_END, stream>>>(p);
}
```

```cpp
#include <hip/hip_runtime.h>
#include <hip/hip_bf16.h>
#include <type_traits>
#include <stdint.h>

#define LAS __attribute__((address_space(3)))
#define GAS __attribute__((address_space(1)))
typedef unsigned short u16;
typedef unsigned long long u64;
typedef short bf16x8 __attribute__((ext_vector_type(8)));
typedef short s16x4 __attribute__((ext_vector_type(4)));
typedef float f32x16 __attribute__((ext_vector_type(16)));
typedef float f32x4 __attribute__((ext_vector_type(4)));
typedef float f32x2 __attribute__((ext_vector_type(2)));
typedef __bf16 bf16x2 __attribute__((ext_vector_type(2)));
typedef unsigned u32x4 __attribute__((ext_vector_type(4)));
typedef unsigned u32x2 __attribute__((ext_vector_type(2)));
typedef __amdgpu_buffer_rsrc_t rsrc_t;

constexpr int S_ = 2048, D_ = 512, F_ = 1792;
constexpr unsigned WS_EPOCH = 0;
constexpr unsigned WS_FLAGA = 4096;
constexpr unsigned WS_TMO = 16384;
constexpr unsigned WS_FLAGB = 32768;
constexpr unsigned WS_FLAGO = 49152;
constexpr unsigned WS_STATS = 65536;
constexpr unsigned WS_CSA = 131072;
constexpr unsigned WS_CSB = 262144;
constexpr unsigned WS_Q = 524288;
constexpr unsigned WS_K = WS_Q + 2097152;
constexpr unsigned WS_V = WS_K + 2097152;
constexpr unsigned WS_ACT = WS_V + 2097152;
constexpr unsigned WS_END = WS_ACT + 64u * 144u * 1024u;
constexpr int CODE_BYTES = 43008;
constexpr int SLOT = 28800, UOFF = 14336 + 128, NSLOT = 5;
constexpr int L_G = NSLOT * SLOT;
constexpr int L_ST = L_G + 16384;
constexpr int L_CS = L_ST + 2048;
constexpr int L_END = L_CS + 1280;
constexpr int LK = 0, LV = 16384;
static_assert(L_END <= 163840, "LDS");


struct Params {
  const u16* x; const u16* wqkv; const u16* wo; const u16* wg; const u16* wu; const u16* wd;
  u16* out; char* ws;
};

template <int I, int N, class Fn> __device__ __forceinline__ void static_for(Fn&& f) {
  if constexpr (I < N) { f(std::integral_constant<int, I>{}); static_for<I + 1, N>(f); }
}
template <int V> using ic = std::integral_constant<int, V>;
__device__ __forceinline__ unsigned rfl(unsigned v) { return __builtin_amdgcn_readfirstlane(v); }
__device__ __forceinline__ float bf2f(unsigned b16) { return __uint_as_float(b16 << 16); }
__device__ __forceinline__ unsigned pk2(float lo, float hi) { f32x2 v = {lo, hi}; return __builtin_bit_cast(unsigned, __builtin_convertvector(v, bf16x2)); }
__device__ __forceinline__ unsigned f2bf(float f) { return pk2(f, 0.f) & 0xffffu; }
__device__ __forceinline__ float rbf(float f) { return bf2f(f2bf(f)); }
#define MFMA(a, b, c) __builtin_amdgcn_mfma_f32_32x32x16_bf16(a, b, c, 0, 0, 0)
#define SB0() __builtin_amdgcn_sched_barrier(0)
#define SGB(mask, n) __builtin_amdgcn_sched_group_barrier(mask, n, 0)
__device__ __forceinline__ rsrc_t mkrs(const void* p, unsigned bytes) {
  const u64 a = (u64)p;
  const u64 u = ((u64)rfl((unsigned)(a >> 32)) << 32) | rfl((unsigned)a);
  return __builtin_amdgcn_make_buffer_rsrc((void*)u, (short)0, (int)rfl(bytes), 0x00020000);
}
__device__ __forceinline__ void dma16(rsrc_t rs, unsigned ldsaddr, unsigned voff, unsigned soff) {
  asm volatile("s_mov_b32 m0, %0\n\ts_nop 0\n\tbuffer_load_dwordx4 %1, %2, %3 offen lds" ::"s"(ldsaddr), "v"(voff), "s"(rs), "s"(soff) : "memory");
}
__device__ __forceinline__ void dma16sc(rsrc_t rs, unsigned ldsaddr, unsigned voff, unsigned soff) {
  asm volatile("s_mov_b32 m0, %0\n\ts_nop 0\n\tbuffer_load_dwordx4 %1, %2, %3 offen sc1 lds" ::"s"(ldsaddr), "v"(voff), "s"(rs), "s"(soff) : "memory");
}
__device__ __forceinline__ void dma4(rsrc_t rs, unsigned ldsaddr, unsigned voff, unsigned soff) {
  asm volatile("s_mov_b32 m0, %0\n\ts_nop 0\n\tbuffer_load_dword %1, %2, %3 offen lds" ::"s"(ldsaddr), "v"(voff), "s"(rs), "s"(soff) : "memory");
}
__device__ __forceinline__ void gdma4(const void* g, unsigned ldsaddr) {
  asm volatile("s_mov_b32 m0, %0\n\ts_nop 0\n\tglobal_load_lds_dword %1, off" ::"s"(ldsaddr), "v"(g) : "memory");
}
__device__ __forceinline__ s16x4 trd(const LAS char* p) {
  typedef short v4i16_t __attribute__((ext_vector_type(4)));
  return __builtin_bit_cast(s16x4, __builtin_amdgcn_ds_read_tr16_b64_v4i16((LAS v4i16_t*)p));
}
__device__ __forceinline__ bf16x8 cat(s16x4 a, s16x4 b) { return (bf16x8){a[0], a[1], a[2], a[3], b[0], b[1], b[2], b[3]}; }
__device__ __forceinline__ float dot2(unsigned a, unsigned b, float c) { return __builtin_amdgcn_fdot2_f32_bf16(__builtin_bit_cast(bf16x2, a), __builtin_bit_cast(bf16x2, b), c, false); }
__device__ __forceinline__ float swap32_add(float v) { auto r = __builtin_amdgcn_permlane32_swap(__float_as_uint(v), __float_as_uint(v), false, false); return __uint_as_float(r[0]) + __uint_as_float(r[1]); }
__device__ __forceinline__ float swap32_max(float v) { auto r = __builtin_amdgcn_permlane32_swap(__float_as_uint(v), __float_as_uint(v), false, false); return __builtin_fmaxf(__uint_as_float(r[0]), __uint_as_float(r[1])); }
template <int N> __device__ __forceinline__ void wait_bar() { asm volatile("s_waitcnt vmcnt(%0)\n\ts_barrier" ::"n"(N) : "memory"); }
template <int N> __device__ __forceinline__ void wait_vm() { asm volatile("s_waitcnt vmcnt(%0)" ::"n"(N) : "memory"); }
__device__ __forceinline__ void lds_bar() { asm volatile("s_waitcnt lgkmcnt(0)\n\ts_barrier" ::: "memory"); }
__device__ __forceinline__ float wave_sum(float v) {
  v += __builtin_bit_cast(float, __builtin_amdgcn_update_dpp(0, __builtin_bit_cast(int, v), 0x111, 0xf, 0xf, true));
  v += __builtin_bit_cast(float, __builtin_amdgcn_update_dpp(0, __builtin_bit_cast(int, v), 0x112, 0xf, 0xf, true));
  v += __builtin_bit_cast(float, __builtin_amdgcn_update_dpp(0, __builtin_bit_cast(int, v), 0x114, 0xf, 0xf, true));
  v += __builtin_bit_cast(float, __builtin_amdgcn_update_dpp(0, __builtin_bit_cast(int, v), 0x118, 0xf, 0xf, true));
  v += __builtin_bit_cast(float, __builtin_amdgcn_update_dpp(0, __builtin_bit_cast(int, v), 0x142, 0xa, 0xf, true));
  v += __builtin_bit_cast(float, __builtin_amdgcn_update_dpp(0, __builtin_bit_cast(int, v), 0x143, 0xc, 0xf, true));
  return __builtin_bit_cast(float, __builtin_amdgcn_readlane(__builtin_bit_cast(int, v), 63));
}
constexpr int AUX_SC1 = 16;
__device__ __forceinline__ void st16sc(rsrc_t rs, u32x4 v, unsigned off) { __builtin_amdgcn_raw_buffer_store_b128(v, rs, off, 0, AUX_SC1); }
__device__ __forceinline__ void st8sc(rsrc_t rs, u32x2 v, unsigned off) { __builtin_amdgcn_raw_buffer_store_b64(v, rs, off, 0, AUX_SC1); }
__device__ __forceinline__ u32x4 ld16sc(rsrc_t rs, unsigned voff, unsigned soff) { return __builtin_amdgcn_raw_buffer_load_b128(rs, voff, soff, AUX_SC1); }
__device__ __forceinline__ u32x2 ld8sc(rsrc_t rs, unsigned voff) { return __builtin_amdgcn_raw_buffer_load_b64(rs, voff, 0, AUX_SC1); }
constexpr unsigned kSpinCap = 1u << 18;
struct Tag { unsigned lo, hi; };
__device__ __forceinline__ void wait_flags(rsrc_t rsW, unsigned off, Tag tag) {
#pragma clang loop unroll(disable)
  for (unsigned spins = 0;; ++spins) {
    const u32x2 v = ld8sc(rsW, off);
    if (__all(v[0] == tag.lo && v[1] == tag.hi)) break;
    if (spins > kSpinCap) { st8sc(rsW, (u32x2){1u, off}, WS_TMO); break; }
    __builtin_amdgcn_s_sleep(2);
  }
  __builtin_amdgcn_fence(__ATOMIC_ACQUIRE, "wavefront");
}
__device__ __forceinline__ u32x4 check_granule(u32x4 g, rsrc_t rsW, unsigned off, Tag tag) {
  if (!__all(g[2] == tag.lo && g[3] == tag.hi)) {
#pragma clang loop unroll(disable)
    for (unsigned spins = 0;; ++spins) {
      g = ld16sc(rsW, off, 0);
      if (__all(g[2] == tag.lo && g[3] == tag.hi)) break;
      if (spins > kSpinCap) { st8sc(rsW, (u32x2){2u, off}, WS_TMO); break; }
      __builtin_amdgcn_s_sleep(2);
    }
  }
  return g;
}

template <int CLS> constexpr int nld() { return CLS == 0 ? 4 : 3; }
enum { OP_GA = 100, OP_GB = 101, OP_OTHER = 102 };
template <int CLS> struct Sched {
  int id[256]; int n; int bar_at[17];
  constexpr void put(int what, int cnt) { for (int i = 0; i < cnt; ++i) id[n++] = what; }
  constexpr Sched() : id{}, n(0), bar_at{} {
    const int nl = nld<CLS>();
    put(0, nl); put(1, nl); put(2, nl);
    bar_at[0] = n;
    put(3, nl); put(4, nl);
    for (int k = 1; k <= 16; ++k) {
      bar_at[k] = n;
      if (k == 4) put(OP_GA, CLS == 0 ? 1 : 2);
      if (k == 12) put(OP_GB, 2);
      if (k == 8) put(OP_OTHER, CLS == 0 ? 4 : 2);
      if (k + 4 < 16) put(k + 4, nl);
      else if (CLS == 1 && k < 16) put(OP_OTHER, k == 14 ? 8 : (k == 15 ? 4 : 6));
    }
  }
  constexpr int younger(int what, int k) const { int last = -1; for (int i = 0; i < bar_at[k]; ++i) if (id[i] == what) last = i; return bar_at[k] - 1 - last; }
};
template <int CLS> constexpr int waitN(int k) {
  constexpr Sched<CLS> S{};
  int n = 63;
  if (k < 16) n = S.younger(k, k);
  if (k == 7) { const int g = S.younger(OP_GA, k); n = g < n ? g : n; }
  if (k == 15) { const int g = S.younger(OP_GB, k); n = g < n ? g : n; }
  if (k == 16 && CLS == 0) n = 0;
  return n > 63 ? 63 : n;
}
template <int N> __device__ __forceinline__ void bar_n() { asm volatile("s_waitcnt vmcnt(%0) lgkmcnt(0)\n\ts_barrier" ::"n"(N) : "memory"); }
constexpr int L_CR = 32768, CRN = 12;

struct Ctx {
  Params p; rsrc_t rsW; Tag tag; unsigned lds0; const LAS char* L; int b, w8, lane;
  int rot;
};
__device__ __forceinline__ int sigma(int s, int rot) { const int t = s + rot; return t >= 28 ? t - 28 : t; }

template <int CLS>
__device__ __forceinline__ void ab_wave(Ctx& X, const int pnl, const int c, const int w, const int tt_c, const int ct_c, u32x4 (&kvq)[6]) {
  const Params& p = X.p; const unsigned lds0 = X.lds0; const LAS char* L = X.L; const int lane = X.lane; const rsrc_t rsW = X.rsW;
  const int l31 = lane & 31, hh = lane >> 5, blk = (lane >> 4) & 1, q4 = (lane & 15) >> 2, pp = lane & 3;
  const int r0 = (pnl + 2 * (c & 3)) & 7;
  rsrc_t rsA, rsB[4];
  unsigned voA[4], voB[4], soA, ldA[4], ldB[4];
  if constexpr (CLS == 0) {
    rsA = mkrs(p.x + (size_t)pnl * 128 * D_, (S_ - pnl * 128) * D_ * 2);
#pragma unroll
    for (int j = 0; j < 4; ++j) { const int par = j & 1; voA[j] = (8 * par + (lane >> 3)) * 1024 + (((lane & 7) ^ ((4 * par + (lane >> 4)) & 7)) << 4) + (j >> 1) * 16384; ldA[j] = (4 * w + j) * 1024; }
    soA = w * 32 * 1024;
  } else {
    rsA = mkrs(p.wqkv + c * 96, (D_ * 3 * D_ - c * 96) * 2);
#pragma unroll
    for (int j = 0; j < 3; ++j) { const int o = (3 * w + j) * 1024 + lane * 16, row = o / 192, ch = (o % 192) >> 4; voA[j] = row * 3072 + ch * 16; ldA[j] = 16384 + (3 * w + j) * 1024; }
    soA = 0;
  }
  auto issueA = [&](auto sc, auto jc) {
    constexpr int s = decltype(sc)::value, j = decltype(jc)::value; constexpr int buf = (s % NSLOT) * SLOT;
    dma16(rsA, lds0 + buf + ldA[j], voA[j], soA + ((s + r0) & 7) * (CLS == 0 ? 128 : 64 * 3072));
  };
  const rsrc_t rsX = mkrs(p.x, S_ * D_ * 2);
  dma16(rsX, lds0 + L_G + X.w8 * 1024, lane * 16, (X.b * 8 + X.w8) * 1024);
  asm volatile("s_mov_b32 m0, %0\n\ts_nop 0\n\tbuffer_load_dword %1, %2, 0 offen sc1 lds" ::"s"(lds0 + L_ST + X.w8 * 256), "v"(WS_EPOCH + (lane & 1) * 4), "s"(rsW) : "memory");
  static_for<0, 3>([&](auto sc) { static_for<0, nld<CLS>()>([&](auto jc) { issueA(sc, jc); }); if constexpr (decltype(sc)::value < 2) asm volatile("s_barrier" ::: "memory"); });
#pragma unroll
  for (int j = 0; j < nld<CLS>(); ++j) {
    const int i = CLS == 0 ? 4 * w + j : 16 + 3 * w + j;
    const bool isg = i < 14; const int ii = isg ? i : i - 14;
    const int o = ii * 1024 + lane * 16, row = o / 224, ch = (o % 224) >> 4;
    voB[j] = row * 3584 + ch * 16; ldB[j] = isg ? ii * 1024 : UOFF + ii * 1024;
    rsB[j] = mkrs((isg ? p.wg : p.wu) + c * 112, (D_ * F_ - c * 112) * 2);
  }
  const rsrc_t rsD = mkrs(p.wd + ct_c * 64, (F_ * D_ - ct_c * 64) * 2);
  const unsigned voC = (lane >> 3) * 1024 + (((lane & 7) ^ ((((lane >> 3) >> 1) & 1) << 2)) << 4);
  auto issueC = [&](auto sc) {
    constexpr int s = decltype(sc)::value;
    const unsigned so = (28 * w + sigma(s, X.rot)) * 16384;
    dma16(rsD, lds0 + L_CR + s * 8192 + w * 2048, voC, so);
    dma16(rsD, lds0 + L_CR + s * 8192 + w * 2048 + 1024, voC, so + 8192);
  };
  auto issue = [&](auto sc, auto jc) {
    constexpr int s = decltype(sc)::value, j = decltype(jc)::value; constexpr int buf = (s % NSLOT) * SLOT;
    if constexpr (s < 8) issueA(sc, jc);
    else dma16(rsB[j], lds0 + buf + ldB[j], voB[j], ((s + r0) & 7) * (64 * 3584));
  };
  {
    wait_vm<3 * nld<CLS>()>();
    {
      const u32x2 ep = *(const LAS u32x2*)(L + L_ST + X.w8 * 256);
      const u64 epoch = (((u64)rfl(ep[1]) << 32) | rfl(ep[0])) + 1;
      X.tag.lo = (unsigned)epoch; X.tag.hi = (unsigned)(epoch >> 32);
    }
    const u32x4 v = *(const LAS u32x4*)(L + L_G + X.w8 * 1024 + lane * 16);
    float e[8];
#pragma unroll
    for (int i = 0; i < 4; ++i) { e[2 * i] = bf2f(v[i] & 0xffffu); e[2 * i + 1] = __uint_as_float(v[i] & 0xffff0000u); }
    const float mean = wave_sum(((e[0] + e[1]) + (e[2] + e[3])) + ((e[4] + e[5]) + (e[6] + e[7]))) * (1.0f / D_);
    float q = 0;
#pragma unroll
    for (int i = 0; i < 8; ++i) { const float d = e[i] - mean; q += d * d; }
    q = wave_sum(q);
    const unsigned mb = f2bf(mean), sb = f2bf(__builtin_amdgcn_sqrtf(rbf(rbf(q * (1.0f / D_)) + 1e-5f)));
    if (lane == 0) st16sc(rsW, (u32x4){mb | (sb << 16), 0u, X.tag.lo, X.tag.hi}, WS_STATS + (X.b * 8 + X.w8) * 16);
  }
  const int tt = w;
  const int xrow = tt * 32 + l31;
  const unsigned xad = xrow * 128 + ((hh ^ ((xrow >> 1) & 7)) << 4);
  constexpr int NTA = CLS == 0 ? 2 : 1, NTB = CLS == 0 ? 4 : 3;
  const unsigned adA = 16384 + (8 * hh + q4) * 192 + blk * 32 + pp * 8 + (CLS == 0 ? 0 : 128);
  const unsigned adB = blk * UOFF + (8 * hh + q4) * 224 + pp * 8 + (CLS == 0 ? 0 : 4 * 32);
  bf16x8 xf[32];
  f32x16 accA[NTA], accB[NTB];
#pragma unroll
  for (int j = 0; j < NTA; ++j) accA[j] = f32x16{};
#pragma unroll
  for (int j = 0; j < NTB; ++j) accB[j] = f32x16{};
  float cs = 0.f, cs1 = 0.f, cs2 = 0.f, cs3 = 0.f;
  const unsigned ONES = 0x3F803F80u;
  const bool pubA = w < NTA, pubB = w < NTB;
  float rinv = 0.f; unsigned nmb = 0;
  bf16x8 wa[3][NTA], wb[2][NTB];
  unsigned curW, curx;
  auto setbase = [&](auto sc) {
    constexpr int s = decltype(sc)::value; constexpr int buf = (s % NSLOT) * SLOT;
    if constexpr (s < 8) { curW = adA + buf; asm volatile("" : "+v"(curW)); curx = xad + buf; asm volatile("" : "+v"(curx)); }
    else { curW = adB + buf; asm volatile("" : "+v"(curW)); }
  };
  auto fetch = [&](auto gc) {
    constexpr int g = decltype(gc)::value; constexpr int ks = g & 3;
    if constexpr (g < 32) {
      xf[g] = *(const LAS bf16x8*)(L + (curx ^ (ks << 5)));
#pragma unroll
      for (int j = 0; j < NTA; ++j) wa[g % 3][j] = cat(trd(L + curW + j * 64 + (16 * ks) * 192), trd(L + curW + j * 64 + (16 * ks + 4) * 192));
    } else {
#pragma unroll
      for (int j = 0; j < NTB; ++j) wb[g & 1][j] = cat(trd(L + curW + j * 32 + (16 * ks) * 224), trd(L + curW + j * 32 + (16 * ks + 4) * 224));
    }
  };
  auto colsum = [&](auto sc) {
    constexpr int s = decltype(sc)::value; constexpr int buf = (s % NSLOT) * SLOT, RS = s < 8 ? 192 : 224;
    const LAS char* a = L + (s < 8 ? adA + w * 64 : adB + w * 32) + buf;
    bf16x8 f[4];
#pragma unroll
    for (int ks = 0; ks < 4; ++ks) f[ks] = cat(trd(a + (16 * ks) * RS), trd(a + (16 * ks + 4) * RS));
#pragma unroll
    for (int ks = 0; ks < 4; ++ks) { const u32x4 u = __builtin_bit_cast(u32x4, f[ks]); cs = dot2(u[0], ONES, cs); cs1 = dot2(u[1], ONES, cs1); cs2 = dot2(u[2], ONES, cs2); cs3 = dot2(u[3], ONES, cs3); }
  };
  auto publish_cs = [&](unsigned base, int tile) {
    const float cf = swap32_add((cs + cs1) + (cs2 + cs3)); cs = 0.f; cs1 = 0.f; cs2 = 0.f; cs3 = 0.f;
    const float nb = __builtin_bit_cast(float, __builtin_amdgcn_update_dpp(0, __builtin_bit_cast(int, cf), 0xF5, 0xf, 0xf, true));
    if (hh == 0 && (lane & 1) == 0) st16sc(rsW, (u32x4){__float_as_uint(cf), __float_as_uint(nb), X.tag.lo, X.tag.hi}, base + tile * 256 + l31 * 8);
  };
  auto sum_cs = [&](unsigned gbase, int pstride, int ncol, int tile0) {
    const int t = w * 64 + lane;
    if (CLS == 1 && w * 64 < ncol) {
      const int tc = t < ncol ? t : ncol - 1;
      float sum = 0.f; bool ok = true;
#pragma unroll
      for (int part = 0; part < 8; ++part) {
        const u32x4 g = *(const LAS u32x4*)(L + L_G + part * pstride + (tc >> 1) * 16);
        ok = ok && g[2] == X.tag.lo && g[3] == X.tag.hi;
        sum += __uint_as_float((tc & 1) ? g[1] : g[0]);
      }
      if (!__all(ok)) {
        sum = 0.f;
#pragma clang loop unroll(disable)
        for (int part = 0; part < 8; ++part) {
          const u32x4 g = check_granule((u32x4){0u, 0u, ~X.tag.lo, 0u}, rsW, gbase + part * pstride + (tc >> 1) * 16, X.tag);
          sum += __uint_as_float((tc & 1) ? g[1] : g[0]);
        }
      }
      if (t < ncol) *(LAS float*)(L + L_CS + tile0 * 128 + t * 4) = sum;
    }
  };
  float cvA[NTA], cvB[NTB];
  auto csfrag = [&](const float cv) {
    const unsigned h0 = f2bf(cv); const float r1 = cv - bf2f(h0); const unsigned h1 = f2bf(r1); const unsigned h2 = f2bf(r1 - bf2f(h1));
    return hh == 0 ? (bf16x8){(short)h0, (short)h1, (short)h2, 0, 0, 0, 0, 0} : (bf16x8){0, 0, 0, 0, 0, 0, 0, 0};
  };
  const unsigned csA = WS_CSA + c * 8192, csB = WS_CSB + c * 16384;
  unsigned fa_off;
  { const int j = lane < 10 ? lane : 0; const int typ = j < 4 ? 1 : (j < 8 ? 2 : 0);
    int pq = j < 8 ? 2 * ct_c - 2 + (j & 3) : 2 * ct_c + (j & 1); pq = pq < 0 ? pq + 2 : pq;
    fa_off = WS_FLAGA + (pq * 16 + (16 * typ + (tt_c >> 1)) / 3) * 8; }
  auto after_bar = [&](auto nc) {
    constexpr int n = decltype(nc)::value;
    if constexpr (n == 4) {
      dma16sc(rsW, lds0 + L_G + X.w8 * 1024, lane * 16, csA + X.w8 * 1024);
      if constexpr (CLS == 1) dma16sc(rsW, lds0 + L_ST + (w & 1) * 1024, lane * 16, WS_STATS + pnl * 2048 + (w & 1) * 1024);
    }
    if constexpr (n == 12) {
      if (X.w8 == 0 && lane == 0) st8sc(rsW, (u32x2){X.tag.lo, X.tag.hi}, WS_FLAGA + (pnl * 16 + c) * 8);
      dma16sc(rsW, lds0 + L_G + X.w8 * 2048, lane * 16, csB + X.w8 * 2048); dma16sc(rsW, lds0 + L_G + X.w8 * 2048 + 1024, lane * 16, csB + X.w8 * 2048 + 1024);
    }
    if constexpr (n == 14) { if (X.w8 == 0) dma16sc(rsW, lds0 + L_ST, fa_off, 0); }
    if constexpr (n == 7) {
      sum_cs(csA, 1024, 96, 0);
      u32x4 stg = *(const LAS u32x4*)(L + L_ST + (tt * 32 + l31) * 16);
      stg = check_granule(stg, rsW, WS_STATS + (pnl * 128 + tt * 32 + l31) * 16, X.tag);
      rinv = __builtin_amdgcn_rcpf(__uint_as_float(stg[0] & 0xffff0000u));
      nmb = (stg[0] & 0xffffu) ^ 0x8000u;
    }
    if constexpr (n == 8) {
#pragma unroll
      for (int j = 0; j < NTA; ++j) cvA[j] = *(const LAS float*)(L + L_CS + ((CLS == 0 ? 0 : 2) + j) * 128 + l31 * 4);
    }
    if constexpr (n == 16) {
#pragma unroll
      for (int j = 0; j < NTB; ++j) cvB[j] = *(const LAS float*)(L + L_CS + (3 + (CLS == 0 ? 0 : 4) + j) * 128 + l31 * 4);
    }
    if constexpr (n == 15) sum_cs(csB, 2048, 224, 3);
  };
  auto refill = [&](auto nc, auto pc) {
    constexpr int n = decltype(nc)::value, pos = decltype(pc)::value;
    if constexpr (n + 4 < 16) { if constexpr (CLS == 0) issue(ic<n + 4>{}, pc); else if constexpr (pos >= 1) issue(ic<n + 4>{}, ic<pos - 1>{}); }
    else if constexpr (CLS == 1) {
      if constexpr (n == 12 && pos >= 1) issueC(ic<pos - 1>{});
      if constexpr (n == 13 && pos >= 1) issueC(ic<pos + 2>{});
      if constexpr (n == 14) issueC(ic<pos + 6>{});
      if constexpr (n == 15 && (pos == 1 || pos == 2)) issueC(ic<pos + 9>{});
    }
  };
  bar_n<waitN<CLS>(0)>();
  static_for<3, 5>([&](auto sc) { static_for<0, nld<CLS>()>([&](auto jc) { issueA(sc, jc); }); });
  setbase(ic<0>{}); fetch(ic<0>{}); fetch(ic<1>{});
  static_for<0, 16>([&](auto sc) {
    constexpr int s = decltype(sc)::value;
    static_for<0, 4>([&](auto kc) {
      constexpr int ks = decltype(kc)::value, g = 4 * s + ks;
      constexpr int bk = s + 1 < 8 ? 2 : 3, pbk = s < 8 ? 2 : 3;
      if constexpr (ks == bk) { bar_n<waitN<CLS>(s + 1)>(); after_bar(ic<s + 1>{}); if constexpr (s + 1 < 16) setbase(ic<s + 1>{}); }
      if constexpr (ks == bk && s == 15) {
        const u32x2 fl = *(const LAS u32x2*)(L + L_ST + lane * 16);
        if (!__all(fl[0] == X.tag.lo && fl[1] == X.tag.hi)) wait_flags(rsW, fa_off, X.tag);
#pragma unroll
        for (int j = 0; j < 2; ++j) {
          int kt = 8 * ct_c - 8 + 2 * X.w8 + j; kt = kt < 0 ? 0 : kt;
          kvq[j] = ld16sc(rsW, lane * 16, WS_K + (tt_c * 64 + kt) * 1024);
          kvq[2 + j] = ld16sc(rsW, lane * 16, WS_V + (tt_c * 64 + kt) * 1024);
        }
        if constexpr (CLS == 0) { kvq[4] = ld16sc(rsW, lane * 16, WS_Q + (tt_c * 64 + 8 * ct_c + w) * 1024); kvq[5] = ld16sc(rsW, lane * 16, WS_Q + (tt_c * 64 + 8 * ct_c + w + 4) * 1024); }
      }
      if constexpr (ks >= bk) refill(ic<s + 1>{}, ic<ks - bk>{});
      else if constexpr (s > 0 && ks + 4 - pbk < 4) refill(ic<s>{}, ic<ks + 4 - pbk>{});
      SB0();
      if constexpr (g + 2 < 32) fetch(ic<g + 2>{});
      if constexpr (g + 1 >= 32 && g + 1 < 64) fetch(ic<g + 1>{});
      if constexpr (g < 32) {
#pragma unroll
        for (int j = 0; j < NTA; ++j) accA[j] = MFMA(wa[g % 3][j], xf[g], accA[j]);
        if constexpr (g < 30) { for (int j = 0; j < NTA; ++j) { SGB(0x8, 1); SGB(0x100, 3); } }
      } else {
#pragma unroll
        for (int j = 0; j < NTB; ++j) accB[j] = MFMA(wb[g & 1][j], xf[g - 32], accB[j]);
        if constexpr (ks == 2) { SGB(0x8, 1); SGB(0x100, NTB); SGB(0x8, 1); SGB(0x100, NTB); SGB(0x8, NTB - 2); }
        else { for (int j = 0; j < NTB; ++j) { SGB(0x8, 1); SGB(0x100, 2); } }
      }
      SB0();
      if constexpr (g == 0) { if (pubA) { colsum(ic<0>{}); publish_cs(csA + r0 * 1024, (CLS == 0 ? 0 : 2) + w); } SB0(); }
      if constexpr (g == 32) { if (pubB) { colsum(ic<8>{}); publish_cs(csB + r0 * 2048, (CLS == 0 ? 0 : 4) + w); } SB0(); }
    });
    if constexpr (s == 7) {
      const bf16x8 mfrag = hh == 0 ? (bf16x8){(short)nmb, (short)nmb, (short)nmb, 0, 0, 0, 0, 0} : (bf16x8){0, 0, 0, 0, 0, 0, 0, 0};
      const int gt = pnl * 4 + tt;
#pragma unroll
      for (int j = 0; j < NTA; ++j) {
        const int tl = (CLS == 0 ? 0 : 2) + j;
        accA[j] = MFMA(csfrag(cvA[j]), mfrag, accA[j]);
        const int T = 3 * c + tl;
        const float sc_ = T < 16 ? rinv * (0.25f * 1.4426950408889634f) : rinv;
#pragma unroll
        for (int hd = 0; hd < 2; ++hd) {
          u32x4 o;
#pragma unroll
          for (int i = 0; i < 4; ++i) o[i] = pk2(accA[j][8 * hd + 2 * i] * sc_, accA[j][8 * hd + 2 * i + 1] * sc_);
          st16sc(rsW, o, WS_Q + ((2 * T + hd) * 64 + gt) * 1024 + lane * 16);
        }
      }
    }
  });
  {
    const bf16x8 mfrag = hh == 0 ? (bf16x8){(short)nmb, (short)nmb, (short)nmb, 0, 0, 0, 0, 0} : (bf16x8){0, 0, 0, 0, 0, 0, 0, 0};
    const int gt = pnl * 4 + tt;
    const float r2 = rinv * rinv;
#pragma unroll
    for (int j = 0; j < NTB; ++j) {
      const int tl = (CLS == 0 ? 0 : 4) + j;
      accB[j] = MFMA(csfrag(cvB[j]), mfrag, accB[j]);
      u32x4 o;
#pragma unroll
      for (int i = 0; i < 4; ++i) o[i] = pk2(accB[j][2 * i] * accB[j][2 * i + 8] * r2, accB[j][2 * i + 1] * accB[j][2 * i + 9] * r2);
      st16sc(rsW, o, WS_ACT + (gt * 144 + 7 * c + tl) * 1024 + lane * 16);
    }
  }
}

__device__ __forceinline__ void stage_kv(const Ctx& X, const u32x4 (&kvq)[6]) {
  const LAS char* L = X.L; const int lane = X.lane, w = X.w8;
  if (w == 0) *(LAS u32x4*)(L + L_G + lane * 16) = (u32x4){0x3F803F80u, 0x3F803F80u, 0x3F803F80u, 0x3F803F80u};
#pragma unroll
  for (int j = 0; j < 2; ++j) {
    *(LAS u32x4*)(L + LK + (2 * w + j) * 1024 + lane * 16) = kvq[j];
    *(LAS u32x4*)(L + LV + (2 * w + j) * 1024 + lane * 16) = kvq[2 + j];
  }
  lds_bar();
}

__device__ __forceinline__ void attn_wave(const Ctx& X, const int pnl, const int c, const int hd, const int qb, const u32x4 (&kvq)[6]) {
  const LAS char* L = X.L; const rsrc_t rsW = X.rsW; const int lane = X.lane, w = X.w8;
  const int l31 = lane & 31, hh = lane >> 5;
  const u32x2 tagv = {X.tag.lo, X.tag.hi};
  const f32x16 zero = f32x16{};
  const float NEG = -1e30f;
  const int tq = l31 - 4 * hh;
  const int blk = (lane >> 4) & 1, q4 = (lane & 15) >> 2, pp = lane & 3;
  const unsigned vs = blk ? 0u : 1024u;
  static constexpr int ORD[9] = {0, 8, 7, 6, 5, 4, 3, 2, 1};
#pragma clang loop unroll(disable)
  for (int it = 0; it < 2; ++it) {
    const int wq = w + 4 * it, qt = 8 * qb + wq;
    const bf16x8 qf = __builtin_bit_cast(bf16x8, it == 0 ? kvq[4] : kvq[5]);
    const int first = qb == 0 ? 8 - wq : 0;
    const int ntile = 9 - (first > 1 ? first - 1 : 0);
    const LAS char* kp = L + LK + wq * 1024 + lane * 16;
    auto kfr = [&](int i) { return *(const LAS bf16x8*)(kp + i * 1024); };
    const unsigned vl = blk ? (unsigned)L_G : (unsigned)(LV + wq * 1024 + (pp & 1) * 512 + (4 * hh + q4) * 16 + (pp >> 1) * 8);
    f32x16 o = zero;
    float mx;
    {
      float a = NEG, b2 = NEG;
      f32x16 s[3]; bf16x8 kf[3];
      kf[0] = kfr(0); kf[1] = kfr(8); kf[2] = kfr(7);
      s[0] = MFMA(kf[0], qf, zero); s[1] = MFMA(kf[1], qf, zero);
      SB0();
      static_for<2, 10>([&](auto jc) {
        constexpr int j = decltype(jc)::value;
        if (j <= ntile) {
          if constexpr (j < 9) s[j % 3] = MFMA(kf[j % 3], qf, zero);
          if constexpr (j + 1 < 9) kf[(j + 1) % 3] = kfr(ORD[j + 1]);
          SB0();
          if constexpr (j == 2) {
#pragma unroll
            for (int r = 0; r < 16; r += 2) {
              const float v0 = ((r & 3) + 8 * (r >> 2) > tq) ? (first > 0 ? NEG : s[0][r]) : s[1][r];
              const float v1 = (((r + 1) & 3) + 8 * (r >> 2) > tq) ? (first > 0 ? NEG : s[0][r + 1]) : s[1][r + 1];
              if (r & 2) b2 = __builtin_fmaxf(__builtin_fmaxf(b2, v0), v1); else a = __builtin_fmaxf(__builtin_fmaxf(a, v0), v1);
            }
          } else {
#pragma unroll
            for (int r = 0; r < 16; r += 4) { a = __builtin_fmaxf(__builtin_fmaxf(a, s[(j - 1) % 3][r]), s[(j - 1) % 3][r + 1]); b2 = __builtin_fmaxf(__builtin_fmaxf(b2, s[(j - 1) % 3][r + 2]), s[(j - 1) % 3][r + 3]); }
          }
          SB0();
        }
      });
      mx = swap32_max(__builtin_fmaxf(a, b2));
    }
    wait_vm<0>();
    if (lane == 0) st8sc(rsW, tagv, it == 0 ? WS_FLAGB + (((pnl * 4 + w) * 16 + c) * 2) * 8 : WS_FLAGO + ((qt - 4) * 32 + hd) * 8);
    {
      f32x16 negm;
#pragma unroll
      for (int r = 0; r < 16; ++r) negm[r] = -mx;
      f32x16 s[3]; bf16x8 kf[3];
      auto vfr = [&](int i, int st) { const LAS char* vp = L + (vl + i * vs) + st * 256; return cat(trd(vp), trd(vp + 128)); };
      kf[0] = kfr(0); kf[1] = kfr(8); kf[2] = kfr(7);
      s[0] = MFMA(kf[0], qf, negm); s[1] = MFMA(kf[1], qf, negm);
      SB0();
      static_for<2, 10>([&](auto jc) {
        constexpr int j = decltype(jc)::value;
        if (j <= ntile) {
          if constexpr (j < 9) s[j % 3] = MFMA(kf[j % 3], qf, negm);
          if constexpr (j + 1 < 9) kf[(j + 1) % 3] = kfr(ORD[j + 1]);
          if constexpr (j == 2) {
            const bf16x8 va0 = vfr(0, 0), va1 = vfr(0, 1), vb0 = vfr(8, 0), vb1 = vfr(8, 1);
            SB0();
            u32x4 a0, a1, b0, b1;
#pragma unroll
            for (int i = 0; i < 8; ++i) {
              const int r = 2 * i, k0 = (r & 3) + 8 * (r >> 2);
              const bool m0 = k0 > tq, m1 = k0 + 1 > tq;
              const float v0 = m0 ? (first > 0 ? NEG : s[0][r]) : s[1][r], v1 = m1 ? (first > 0 ? NEG : s[0][r + 1]) : s[1][r + 1];
              const unsigned pk = pk2(__builtin_amdgcn_exp2f(v0), __builtin_amdgcn_exp2f(v1));
              const unsigned msk = m0 ? 0xffffffffu : (m1 ? 0xffff0000u : 0u);
              if (i < 4) { a0[i] = pk & msk; b0[i] = pk & ~msk; } else { a1[i - 4] = pk & msk; b1[i - 4] = pk & ~msk; }
            }
            o = MFMA(va0, __builtin_bit_cast(bf16x8, a0), o); o = MFMA(vb0, __builtin_bit_cast(bf16x8, b0), o);
            o = MFMA(va1, __builtin_bit_cast(bf16x8, a1), o); o = MFMA(vb1, __builtin_bit_cast(bf16x8, b1), o);
          } else {
            constexpr int i = ORD[j - 1];
            const bf16x8 v0 = vfr(i, 0), v1 = vfr(i, 1);
            SB0();
            f32x16& t = s[(j - 1) % 3];
            u32x4 p0, p1;
#pragma unroll
            for (int r = 0; r < 8; ++r) t[r] = __builtin_amdgcn_exp2f(t[r]);
#pragma unroll
            for (int r = 0; r < 4; ++r) p0[r] = pk2(t[2 * r], t[2 * r + 1]);
#pragma unroll
            for (int r = 8; r < 10; ++r) t[r] = __builtin_amdgcn_exp2f(t[r]);
            o = MFMA(v0, __builtin_bit_cast(bf16x8, p0), o);
            SB0();
#pragma unroll
            for (int r = 10; r < 16; ++r) t[r] = __builtin_amdgcn_exp2f(t[r]);
#pragma unroll
            for (int r = 0; r < 4; ++r) p1[r] = pk2(t[8 + 2 * r], t[9 + 2 * r]);
            o = MFMA(v1, __builtin_bit_cast(bf16x8, p1), o);
          }
          SB0();
        }
      });
    }
    const float li = 1.0f / o[8];
    u32x4 ov;
#pragma unroll
    for (int i = 0; i < 4; ++i) ov[i] = pk2(o[2 * i] * li, o[2 * i + 1] * li);
    st16sc(rsW, ov, WS_ACT + (qt * 144 + 112 + hd) * 1024 + lane * 16);
  }
  wait_vm<0>();
  if (lane == 0) st8sc(rsW, tagv, WS_FLAGO + ((8 * qb + w + 4) * 32 + hd) * 8);
  if (X.b == 0 && w == 0) {
#pragma clang loop unroll(disable)
    for (int j = 0; j < 4; ++j) wait_flags(rsW, WS_FLAGA + (j * 64 + lane) * 8, X.tag);
    if (lane == 0) st8sc(rsW, tagv, WS_EPOCH);
  }
}

constexpr int O_AT = 22;
constexpr int ACT0 = 5; constexpr int WRN = 10;
constexpr int act_hi(int s) {
  int h = ACT0 + 2 * (s + 1); return h > 28 ? 28 : h;
}
__device__ __forceinline__ void c_wave(const Ctx& X, const int pnl, const int c, const int tt, const int ct, f32x16 (&acc)[2][2]) {
  const Params& p = X.p; const unsigned lds0 = X.lds0; const LAS char* L = X.L; const rsrc_t rsW = X.rsW; const int lane = X.lane, w = X.w8 - 4;
  const int hh = lane >> 5, blk = (lane >> 4) & 1, q4 = (lane & 15) >> 2, pp = lane & 3;
  const u32x2 tagv = {X.tag.lo, X.tag.hi};
  const rsrc_t rsD = mkrs(p.wd + ct * 64, (F_ * D_ - ct * 64) * 2), rsO = mkrs(p.wo + ct * 64, (D_ * D_ - ct * 64) * 2);
  const unsigned vo = (lane >> 3) * 1024 + (((lane & 7) ^ ((((lane >> 3) >> 1) & 1) << 2)) << 4);
  wait_vm<0>();
  if (lane == 0) st8sc(rsW, tagv, WS_FLAGB + (((pnl * 4 + w) * 16 + c) * 2 + 1) * 8);
  u32x4 wr[WRN][2];
  static_for<12, 12 + WRN>([&](auto sc) {
    constexpr int s = decltype(sc)::value;
    const unsigned so = (28 * w + sigma(s, X.rot)) * 16384;
    wr[s - 12][0] = __builtin_amdgcn_raw_buffer_load_b128(rsD, vo, so, 0); wr[s - 12][1] = __builtin_amdgcn_raw_buffer_load_b128(rsD, vo, so + 8192, 0);
  });
  wait_flags(rsW, WS_FLAGB + tt * 512 + (((lane >> 3) & 1) * 32 + (4 * w + ((lane >> 1) & 3)) * 2 + (lane & 1)) * 8, X.tag);
  u32x4 act[36][2];
  auto load_act = [&](auto lo, auto hi) {
    static_for<decltype(lo)::value, decltype(hi)::value>([&](auto sc) {
      constexpr int s = decltype(sc)::value;
      const int ks = s < 28 ? 28 * w + sigma(s, X.rot) : 112 + 8 * w + (s - 28);
#pragma unroll
      for (int tk = 0; tk < 2; ++tk) act[s][tk] = ld16sc(rsW, lane * 16, WS_ACT + ((2 * tt + tk) * 144 + ks) * 1024);
    });
  };
  load_act(ic<0>{}, ic<ACT0>{});
  const unsigned ra0 = L_CR + w * 2048 + (4 * hh + q4) * 128 + (((2 * blk + (pp >> 1)) ^ (((q4 >> 1) & 1) << 2)) << 4) + (pp & 1) * 8;
  unsigned rb[2][2];
  rb[0][0] = ra0; rb[1][0] = ra0 ^ 64; rb[0][1] = ra0 + 6 * 8192; rb[1][1] = (ra0 ^ 64) + 6 * 8192;
#pragma unroll
  for (int i = 0; i < 2; ++i)
#pragma unroll
    for (int j = 0; j < 2; ++j) acc[i][j] = f32x16{};
  static_for<0, 36>([&](auto sc) {
    constexpr int s = decltype(sc)::value;
    if constexpr (s - CRN >= O_AT) wait_vm<(35 - s) * 2>();
    static_assert(12 + WRN >= O_AT, "W(12 + WRN ..) must not be needed before the drain at O_AT");
    if constexpr (s == O_AT) {
      wait_flags(rsW, WS_FLAGO + tt * 512 + (((lane >> 3) & 1) * 32 + 8 * w + (lane & 7)) * 8, X.tag);
      load_act(ic<28>{}, ic<36>{});
    }
    constexpr int row = s % CRN, hf = row / 6, ro = (row % 6) * 8192;
    const bf16x8 w0 = cat(trd(L + rb[0][hf] + ro), trd(L + rb[0][hf] + ro + 1024));
    const bf16x8 w1 = cat(trd(L + rb[1][hf] + ro), trd(L + rb[1][hf] + ro + 1024));
    acc[0][0] = MFMA(w0, __builtin_bit_cast(bf16x8, act[s][0]), acc[0][0]);
    acc[0][1] = MFMA(w0, __builtin_bit_cast(bf16x8, act[s][1]), acc[0][1]);
    acc[1][0] = MFMA(w1, __builtin_bit_cast(bf16x8, act[s][0]), acc[1][0]);
    acc[1][1] = MFMA(w1, __builtin_bit_cast(bf16x8, act[s][1]), acc[1][1]);
    if constexpr (s < WRN) {
      *(LAS u32x4*)(L + L_CR + row * 8192 + w * 2048 + lane * 16) = wr[s][0];
      *(LAS u32x4*)(L + L_CR + row * 8192 + w * 2048 + 1024 + lane * 16) = wr[s][1];
    } else if constexpr (s + CRN < 36) {
      asm volatile("s_waitcnt lgkmcnt(0)" ::: "memory");
      const unsigned d = lds0 + L_CR + row * 8192 + w * 2048;
      if constexpr (s + CRN < 28) { const unsigned so = (28 * w + sigma(s + CRN, X.rot)) * 16384; dma16(rsD, d, vo, so); dma16(rsD, d + 1024, vo, so + 8192); }
      else { dma16(rsO, d, vo, (8 * w + s + CRN - 28) * 16384); dma16(rsO, d + 1024, vo, (8 * w + s + CRN - 28) * 16384 + 8192); }
    }
    if constexpr (act_hi(s) > (s == 0 ? ACT0 : act_hi(s - 1))) load_act(ic<(s == 0 ? ACT0 : act_hi(s - 1))>{}, ic<act_hi(s)>{});
    __builtin_amdgcn_sched_barrier(0);
  });
}

__device__ __forceinline__ void finish(const Ctx& X, const int tt, const int ct, const f32x16 (&acc)[2][2], const bool writer) {
  const Params& p = X.p; const LAS char* L = X.L; const int lane = X.lane, tid = X.w8 * 64 + lane;
  const u32x4 xres = *(const u32x4*)(p.x + (size_t)(tt * 64 + (tid >> 3)) * D_ + ct * 64 + (tid & 7) * 8);
  lds_bar();
  if (writer) {
    const int l31 = lane & 31, hh = lane >> 5, w = X.w8 - 4;
#pragma unroll
    for (int nt = 0; nt < 2; ++nt)
#pragma unroll
      for (int tk = 0; tk < 2; ++tk)
#pragma unroll
        for (int g = 0; g < 4; ++g) {
          const int token = 32 * tk + l31, ch = 8 * nt + 2 * g + hh;
          *(LAS f32x4*)(L + L_CR + w * 16384 + token * 256 + ((ch ^ (token & 15)) << 4)) = (f32x4){acc[nt][tk][4 * g], acc[nt][tk][4 * g + 1], acc[nt][tk][4 * g + 2], acc[nt][tk][4 * g + 3]};
        }
  }
  lds_bar();
  const int token = tid >> 3, c8 = tid & 7;
  f32x4 s0 = {0, 0, 0, 0}, s1 = {0, 0, 0, 0};
#pragma unroll
  for (int ww = 0; ww < 4; ++ww) {
    s0 += *(const LAS f32x4*)(L + L_CR + ww * 16384 + token * 256 + (((2 * c8) ^ (token & 15)) << 4));
    s1 += *(const LAS f32x4*)(L + L_CR + ww * 16384 + token * 256 + (((2 * c8 + 1) ^ (token & 15)) << 4));
  }
  u32x4 ow;
  ow[0] = pk2(s0[0] + bf2f(xres[0] & 0xffffu), s0[1] + __uint_as_float(xres[0] & 0xffff0000u));
  ow[1] = pk2(s0[2] + bf2f(xres[1] & 0xffffu), s0[3] + __uint_as_float(xres[1] & 0xffff0000u));
  ow[2] = pk2(s1[0] + bf2f(xres[2] & 0xffffu), s1[1] + __uint_as_float(xres[2] & 0xffff0000u));
  ow[3] = pk2(s1[2] + bf2f(xres[3] & 0xffffu), s1[3] + __uint_as_float(xres[3] & 0xffff0000u));
  *(u32x4*)(p.out + (size_t)(tt * 64 + token) * D_ + ct * 64 + c8 * 8) = ow;
}

__global__ __launch_bounds__(512, 2) void k_fused(Params p) {
  extern __shared__ __attribute__((aligned(16))) char smem[];
  Ctx X;
  X.p = p; X.b = blockIdx.x; X.w8 = rfl(threadIdx.x >> 6); X.lane = threadIdx.x & 63;
  X.lds0 = (unsigned)(size_t)smem; X.L = (const LAS char*)smem;
  X.rsW = mkrs(p.ws, WS_END);

  const int xcd = X.b & 7, r = X.b >> 3;
  {
    const u64 pc = __builtin_amdgcn_s_getpc() & ~255ull;
    const rsrc_t rsC = __builtin_amdgcn_make_buffer_rsrc((void*)pc, (short)0, CODE_BYTES, 0x00020000);
    if (X.w8 == 7) dma4(rsC, X.lds0 + L_CS, (r * 16 + (X.lane & 15)) * 128, 0);
  }
  {
    if (X.w8 == 6) {
      const int j = X.lane & 15;
      const char* a = j == 0 ? (const char*)p.wg : j == 1 ? (const char*)p.wu : j == 2 ? (const char*)p.wd : j == 3 ? (const char*)p.wo : j == 4 ? (const char*)p.out
                    : j == 5 ? (const char*)p.wg + 1835008 - 128 : j == 6 ? (const char*)p.wu + 1835008 - 128 : j == 7 ? (const char*)p.wd + 1835008 - 128
                    : p.ws + (size_t)(j - 8) * 2097152;
      gdma4(a, X.lds0 + L_CS + 256);
    }
  }
  const int pnl = (xcd >> 2) * 8 + (r & 7), c = (xcd & 3) * 4 + (r >> 3);
  const int u = xcd * 4 + (r & 3), v = r >> 2;
  { const int t = ((v * 7) >> 1) + (u & 3) * 7; X.rot = t >= 28 ? t - 28 : t; }
  u32x4 kvq[6];
  f32x16 acc[2][2];
  if (X.w8 < 4) {
    ab_wave<0>(X, pnl, c, X.w8, u, v, kvq);
    stage_kv(X, kvq);
    attn_wave(X, pnl, c, u, v, kvq);
    finish(X, u, v, acc, false);
  } else {
    ab_wave<1>(X, pnl, c, X.w8 - 4, u, v, kvq);
    stage_kv(X, kvq);
    c_wave(X, pnl, c, u, v, acc);
    finish(X, u, v, acc, true);
  }
}

extern "C" void kernel_launch(void* const* d_in, const int* in_sizes, int n_in, void* d_out, int out_size, void* d_ws, size_t ws_size, hipStream_t stream) {
  Params p;
  p.x = (const u16*)d_in[0]; p.wqkv = (const u16*)d_in[1]; p.wo = (const u16*)d_in[2];
  p.wg = (const u16*)d_in[4]; p.wu = (const u16*)d_in[5]; p.wd = (const u16*)d_in[6];
  p.out = (u16*)d_out; p.ws = (char*)d_ws;
  (void)hipFuncSetAttribute((const void*)k_fused, hipFuncAttributeMaxDynamicSharedMemorySize, L_END);
  k_fused<<<256, 512, L_END, stream>>>(p);
}
```
